```python
import torch

def _softmax(x, axis=-1):
    return torch.softmax(x, dim=axis)
def _gelu(x):
    return torch.nn.functional.gelu(x, approximate="tanh")
def _silu(x):
    return torch.nn.functional.silu(x)
def _sigmoid(x):
    return torch.sigmoid(x)
def _layernorm(x, axis=-1, eps=1e-5):
    m = x.mean(dim=axis, keepdim=True)
    v = x.var(dim=axis, keepdim=True, unbiased=False)
    return (x - m) / torch.sqrt(v + eps)
def _scatter_add(tgt, idx, src, axis=0):
    return tgt.clone().index_add_(axis, idx, src)
def _segment_sum(src, seg, n):
    out = torch.zeros((n,) + src.shape[1:], dtype=src.dtype)
    return out.index_add_(0, seg, src)
def _pad(x, pw):
    flat = tuple(v for pair in reversed(pw) for v in pair)
    return torch.nn.functional.pad(x, flat)

kernel_name = 'concat_heads__gqa_block_cached__gqa_block_cached__near_ridge'

def setup_inputs():

    _w = (lambda a: (lambda t: t.to(torch.bfloat16) if t.is_floating_point() else t)(torch.as_tensor(a)))
    def _A_setup():
        B, Sq, D = 8, 128, 640
        Hq, Hkv, Sk, Dh = 32, 32, 512, 192
        assert Hq % Hkv == 0, (Hq, Hkv)
        return {
            "x": _w((lambda *s: torch.randn(*s, dtype=torch.bfloat16) * 0.8)(B, Sq, D)),
            "k_cache": _w((lambda *s: torch.randn(*s, dtype=torch.bfloat16) * 0.8)(B, Hkv, Sk, Dh)),
            "v_cache": _w((lambda *s: torch.randn(*s, dtype=torch.bfloat16) * 0.05)(B, Hkv, Sk, Dh)),
            "wq": _w((lambda *s: torch.randn(*s, dtype=torch.bfloat16) * 0.8)(D, Hq * Dh) * (D ** -0.5)),
            "wo": _w((lambda *s: torch.randn(*s, dtype=torch.bfloat16) * 0.05)(Hq * Dh, D) * ((Hq * Dh) ** -0.5)),
        }
    _A_in = _A_setup()

    def _B_setup():
        B, Sq, D = 8, 128, 640
        Hq, Hkv, Sk, Dh = 32, 32, 512, 192
        assert Hq % Hkv == 0, (Hq, Hkv)
        return {
            "x": _w((lambda *s: torch.randn(*s, dtype=torch.bfloat16) * 0.8)(B, Sq, D)),
            "k_cache": _w((lambda *s: torch.randn(*s, dtype=torch.bfloat16) * 0.8)(B, Hkv, Sk, Dh)),
            "v_cache": _w((lambda *s: torch.randn(*s, dtype=torch.bfloat16) * 0.05)(B, Hkv, Sk, Dh)),
            "wq": _w((lambda *s: torch.randn(*s, dtype=torch.bfloat16) * 0.8)(D, Hq * Dh) * (D ** -0.5)),
            "wo": _w((lambda *s: torch.randn(*s, dtype=torch.bfloat16) * 0.05)(Hq * Dh, D) * ((Hq * Dh) ** -0.5)),
        }
    _B_in = _B_setup()

    return {
        "A_x": _A_in["x"],
        "A_k_cache": _A_in["k_cache"],
        "A_v_cache": _A_in["v_cache"],
        "A_wq": _A_in["wq"],
        "A_wo": _A_in["wo"],
        "B_x": _B_in["x"],
        "B_k_cache": _B_in["k_cache"],
        "B_v_cache": _B_in["v_cache"],
        "B_wq": _B_in["wq"],
        "B_wo": _B_in["wo"],
    }

def reference(A_x, A_k_cache, A_v_cache, A_wq, A_wo, B_x, B_k_cache, B_v_cache, B_wq, B_wo):
    def _A_ref(x, k_cache, v_cache, wq, wo):
        B, Sq, D = x.shape
        Hq, Dh = 32, 192
        Hkv = k_cache.shape[1]
        rep = Hq // Hkv
        q = (lambda x, *d: x.permute(*d))((x @ wq).reshape(B, Sq, Hq, Dh), 0, 2, 1, 3)
        k = (lambda x, n, axis: torch.repeat_interleave(x, n, dim=axis))(k_cache, rep, axis=1)
        v = (lambda x, n, axis: torch.repeat_interleave(x, n, dim=axis))(v_cache, rep, axis=1)
        scores = torch.einsum("bhqd,bhkd->bhqk", q, k) * (Dh ** -0.5)
        p = _softmax(scores, axis=-1)
        o = torch.einsum("bhqk,bhkd->bhqd", p, v)
        return (lambda x, *d: x.permute(*d))(o, 0, 2, 1, 3).reshape(B, Sq, Hq * Dh) @ wo

    def _B_ref(x, k_cache, v_cache, wq, wo):
        B, Sq, D = x.shape
        Hq, Dh = 32, 192
        Hkv = k_cache.shape[1]
        rep = Hq // Hkv
        q = (lambda x, *d: x.permute(*d))((x @ wq).reshape(B, Sq, Hq, Dh), 0, 2, 1, 3)
        k = (lambda x, n, axis: torch.repeat_interleave(x, n, dim=axis))(k_cache, rep, axis=1)
        v = (lambda x, n, axis: torch.repeat_interleave(x, n, dim=axis))(v_cache, rep, axis=1)
        scores = torch.einsum("bhqd,bhkd->bhqk", q, k) * (Dh ** -0.5)
        p = _softmax(scores, axis=-1)
        o = torch.einsum("bhqk,bhkd->bhqd", p, v)
        return (lambda x, *d: x.permute(*d))(o, 0, 2, 1, 3).reshape(B, Sq, Hq * Dh) @ wo

    a = _A_ref(A_x, A_k_cache, A_v_cache, A_wq, A_wo)
    b = _B_ref(B_x, B_k_cache, B_v_cache, B_wq, B_wo)
    return torch.concatenate([a, b], axis=-1)
```

```cpp
#include <hip/hip_runtime.h>
#include <hip/hip_bf16.h>
#include <stdint.h>

typedef __attribute__((ext_vector_type(8))) short bf16x8;
typedef __attribute__((ext_vector_type(4))) short s16x4;
typedef __attribute__((ext_vector_type(16))) float f32x16;
typedef __attribute__((ext_vector_type(4))) float f32x4;
typedef __attribute__((ext_vector_type(4))) unsigned u32x4;
typedef __attribute__((address_space(3))) const char* lds_cptr;
#define DI __device__ __forceinline__
#define MFMA(a, b, c) __builtin_amdgcn_mfma_f32_32x32x16_bf16((a), (b), (c), 0, 0, 0)

constexpr int NB = 8, SQ = 128, DM = 640, NH = 32, SK = 512, DH = 192;
constexpr float QSCALE = 0.07216878364870322f * 1.4426950408889634f;
constexpr float THR = 8.f;

DI unsigned cvtpk(float lo, float hi) { unsigned r; asm("v_cvt_pk_bf16_f32 %0, %1, %2" : "=v"(r) : "v"(lo), "v"(hi)); return r; }
DI bf16x8 pack8(const f32x16& x, int s, float m) {
  u32x4 w = {cvtpk(x[8 * s] * m, x[8 * s + 1] * m), cvtpk(x[8 * s + 2] * m, x[8 * s + 3] * m), cvtpk(x[8 * s + 4] * m, x[8 * s + 5] * m), cvtpk(x[8 * s + 6] * m, x[8 * s + 7] * m)};
  return __builtin_bit_cast(bf16x8, w);
}
DI bf16x8 lds_rd128(lds_cptr p) { return *(const __attribute__((address_space(3))) bf16x8*)p; }
DI s16x4 lds_tr(lds_cptr p) { return __builtin_bit_cast(s16x4, __builtin_amdgcn_ds_read_tr16_b64_v4i16((__attribute__((address_space(3))) s16x4*)p)); }
DI bf16x8 cat(s16x4 a, s16x4 b) { return __builtin_shufflevector(a, b, 0, 1, 2, 3, 4, 5, 6, 7); }
typedef __amdgpu_buffer_rsrc_t srd_t;
DI srd_t mksrd(const void* p, unsigned bytes) { return __builtin_amdgcn_make_buffer_rsrc((void*)p, (short)0, (int)bytes, 0x00020000); }
DI void dma16(unsigned ldsaddr, unsigned voff, srd_t srd, unsigned soff) {
  asm volatile("s_mov_b32 m0, %0\n\ts_nop 0\n\tbuffer_load_dwordx4 %1, %2, %3 offen lds" ::"s"(ldsaddr), "v"(voff), "s"(srd), "s"(soff) : "memory");
}
#define WAIT_BAR(N) asm volatile("s_waitcnt vmcnt(" #N ") lgkmcnt(0)\n\ts_barrier" ::: "memory")
DI unsigned rfl(unsigned x) { return __builtin_amdgcn_readfirstlane(x); }

struct Params {
  const void* x[2]; const void* k[2]; const void* v[2]; const void* wq[2]; const void* wo[2];
  void* ows; float* part; void* out;
};

__global__ __launch_bounds__(256, 2) void k1_attn(Params p) {
  extern __shared__ __attribute__((aligned(16))) char smem[];
  const int tid = threadIdx.x, lane = tid & 63, r = lane & 31, hh = lane >> 5;
  const unsigned w = rfl(tid >> 6);
  const int bid = blockIdx.x, xg = bid & 7, idx = bid >> 3;
  const int br = xg >> 2, h = (xg & 3) * 8 + (idx & 7), b = idx >> 3;
  const unsigned lds0 = (unsigned)(uintptr_t)smem;
  lds_cptr L = (lds_cptr)smem;
  const srd_t sx = mksrd((const char*)p.x[br] + (size_t)b * SQ * DM * 2, SQ * DM * 2);
  const srd_t swq = mksrd((const char*)p.wq[br] + h * DH * 2, DM * NH * DH * 2 - h * DH * 2);
  const srd_t sk = mksrd((const char*)p.k[br] + (size_t)(b * NH + h) * SK * DH * 2, SK * DH * 2);
  const srd_t sv = mksrd((const char*)p.v[br] + (size_t)(b * NH + h) * SK * DH * 2, SK * DH * 2);

  unsigned wq_off[3], x_off[2], k_off[3], v_off[3];
#pragma unroll
  for (int i = 0; i < 3; ++i) {
    const int pp = 64 * i + lane, row = pp / 24, pos = pp % 24;
    wq_off[i] = row * (NH * DH * 2) + (pos ^ (((row >> 1) & 1) << 2)) * 16;
    v_off[i] = row * 384 + (pos ^ (((row >> 1) & 1) << 2)) * 16;
    const int f = (4 * w + (row >> 1)) & 7;
    k_off[i] = row * 384 + ((pos & ~7) | ((pos & 7) ^ f)) * 16;
  }
#pragma unroll
  for (int i = 0; i < 2; ++i) x_off[i] = (lane >> 3) * (DM * 2) + (((lane & 7) ^ ((4 * i + (lane >> 4)) & 7)) * 16);

  auto issue_q = [&](int c, int buf) {
    const unsigned base = lds0 + buf * 40960;
#pragma unroll
    for (int i = 0; i < 6; ++i)
      dma16(base + w * 6144 + i * 1024, wq_off[i % 3], swq, (unsigned)((c * 64 + 16 * w + 8 * (i / 3)) * (NH * DH * 2)));
#pragma unroll
    for (int i = 0; i < 4; ++i)
      dma16(base + 24576 + w * 4096 + i * 1024, x_off[i & 1], sx, (unsigned)((32 * w + 8 * i) * (DM * 2) + c * 128));
  };
  const int blk = (lane >> 4) & 1, q4 = (lane & 15) >> 2, p4 = lane & 3, bit = (q4 >> 1) & 1;
  const int trlo = blk * 32 + (p4 & 1) * 16 + (p4 >> 1) * 8;
  int wq_rd[2], x_rd[4];
#pragma unroll
  for (int v = 0; v < 2; ++v) wq_rd[v] = (8 * hh + q4) * 384 + (v ^ bit) * 64 + trlo;
#pragma unroll
  for (int s = 0; s < 4; ++s) x_rd[s] = 24576 + (32 * w + r) * 128 + (((2 * s + hh) ^ ((r >> 1) & 7)) * 16);

  f32x16 qa[6];
#pragma unroll
  for (int i = 0; i < 6; ++i) qa[i] = f32x16{};
  issue_q(0, 0);
  for (int c = 0; c < 10; ++c) {
    const int buf = c & 1;
    WAIT_BAR(0);
    if (c + 1 < 10) issue_q(c + 1, buf ^ 1);
    lds_cptr Lb = L + buf * 40960;
#pragma unroll
    for (int s = 0; s < 4; ++s) {
      const bf16x8 xb = lds_rd128(Lb + x_rd[s]);
#pragma unroll
      for (int dt = 0; dt < 6; ++dt) {
        lds_cptr a = Lb + wq_rd[dt & 1] + (dt >> 1) * 128 + 16 * s * 384;
        const bf16x8 af = cat(lds_tr(a), lds_tr(a + 4 * 384));
        qa[dt] = MFMA(af, xb, qa[dt]);
      }
    }
  }
  bf16x8 qf[12];
#pragma unroll
  for (int dt = 0; dt < 6; ++dt) { qf[2 * dt] = pack8(qa[dt], 0, QSCALE); qf[2 * dt + 1] = pack8(qa[dt], 1, QSCALE); }

  auto issue_kv = [&](int t, int slot) {
#pragma unroll
    for (int i = 0; i < 3; ++i) dma16(lds0 + slot * 12288 + w * 3072 + i * 1024, k_off[i], sk, (unsigned)(t * 12288 + w * 3072));
#pragma unroll
    for (int i = 0; i < 3; ++i) dma16(lds0 + 36864 + slot * 12288 + w * 3072 + i * 1024, v_off[i], sv, (unsigned)(t * 12288 + w * 3072));
  };
  int k_rd[4], v_rd[2];
#pragma unroll
  for (int s = 0; s < 4; ++s) k_rd[s] = r * 384 + (((2 * s + hh) ^ ((r >> 1) & 7)) * 16);
#pragma unroll
  for (int v = 0; v < 2; ++v) v_rd[v] = 36864 + (4 * hh + q4) * 384 + (v ^ bit) * 64 + trlo;

  WAIT_BAR(0);
  issue_kv(0, 0); issue_kv(1, 1);
  f32x16 o[6];
#pragma unroll
  for (int i = 0; i < 6; ++i) o[i] = f32x16{};
  float m = 0.f, l = 0.f;
  int slot = 0;
#pragma unroll 1
  for (int t = 0; t < 16; ++t) {
    if (t + 1 < 16) WAIT_BAR(6); else WAIT_BAR(0);
    const int nslot = slot == 0 ? 2 : slot - 1;
    if (t + 2 < 16) issue_kv(t + 2, nslot);
    lds_cptr Lk = L + slot * 12288;
    f32x16 s = f32x16{};
#pragma unroll
    for (int ks = 0; ks < 12; ++ks) s = MFMA(lds_rd128(Lk + k_rd[ks & 3] + (ks >> 2) * 128), qf[ks], s);
    float mx = fmaxf(fmaxf(s[0], s[1]), s[2]);
#pragma unroll
    for (int i = 3; i < 15; i += 2) mx = fmaxf(fmaxf(mx, s[i]), s[i + 1]);
    mx = fmaxf(mx, s[15]);
    { auto rr = __builtin_amdgcn_permlane32_swap(__float_as_uint(mx), __float_as_uint(mx), false, false);
      mx = fmaxf(__uint_as_float(rr[0]), __uint_as_float(rr[1])); }
    if (t == 0) { m = mx; }
    else if (__builtin_expect(__any(mx - m > THR), 0)) {
      const float mn = fmaxf(m, mx), f = __builtin_amdgcn_exp2f(m - mn);
      m = mn; l *= f;
#pragma unroll
      for (int d = 0; d < 6; ++d)
#pragma unroll
        for (int i = 0; i < 16; ++i) o[d][i] *= f;
    }
    float ps = 0.f;
#pragma unroll
    for (int i = 0; i < 16; ++i) { s[i] = __builtin_amdgcn_exp2f(s[i] - m); ps += s[i]; }
    l += ps;
    const bf16x8 pf0 = pack8(s, 0, 1.f), pf1 = pack8(s, 1, 1.f);
#pragma unroll
    for (int dt = 0; dt < 6; ++dt) {
      lds_cptr a = Lk + v_rd[dt & 1] + (dt >> 1) * 128;
      o[dt] = MFMA(cat(lds_tr(a), lds_tr(a + 8 * 384)), pf0, o[dt]);
      o[dt] = MFMA(cat(lds_tr(a + 16 * 384), lds_tr(a + 24 * 384)), pf1, o[dt]);
    }
    slot = slot == 2 ? 0 : slot + 1;
  }
  { auto rr = __builtin_amdgcn_permlane32_swap(__float_as_uint(l), __float_as_uint(l), false, false);
    l = __uint_as_float(rr[0]) + __uint_as_float(rr[1]); }
  const float inv = 1.0f / l;
  char* ob = (char*)p.ows + ((size_t)((br * NB + b) * NH + h) * 4 + w) * 12288 + lane * 16;
#pragma unroll
  for (int dt = 0; dt < 6; ++dt) {
    *(bf16x8*)(ob + (2 * dt) * 1024) = pack8(o[dt], 0, inv);
    *(bf16x8*)(ob + (2 * dt + 1) * 1024) = pack8(o[dt], 1, inv);
  }
}

__global__ __launch_bounds__(256, 2) void k2_oproj(Params p) {
  extern __shared__ __attribute__((aligned(16))) char smem[];
  const int tid = threadIdx.x, lane = tid & 63, hh = lane >> 5;
  const unsigned w = rfl(tid >> 6);
  const int bid = blockIdx.x, xg = bid & 7, idx = bid >> 3;
  const int br = xg >> 2, ks8 = (xg & 3) * 2 + (idx & 1), nt = (idx >> 1) & 3, b = idx >> 3;
  const int h0 = ks8 * 4;
  const unsigned lds0 = (unsigned)(uintptr_t)smem;
  lds_cptr L = (lds_cptr)smem;
  const srd_t swo = mksrd((const char*)p.wo[br] + (size_t)h0 * DH * DM * 2 + nt * 320, 768 * DM * 2 - nt * 320);
  unsigned wo_off[5];
#pragma unroll
  for (int i = 0; i < 5; ++i) { const int pp = 64 * i + lane; wo_off[i] = (pp / 20) * (DM * 2) + (pp % 20) * 16; }
  auto issue = [&](int c, int buf) {
#pragma unroll
    for (int i = 0; i < 5; ++i) dma16(lds0 + buf * 20480 + w * 5120 + i * 1024, wo_off[i], swo, (unsigned)((c * 64 + 16 * w) * (DM * 2)));
  };
  const int blk = (lane >> 4) & 1, q4 = (lane & 15) >> 2, p4 = lane & 3;
  const int wo_rd = (8 * hh + q4) * 320 + blk * 32 + (p4 & 1) * 16 + (p4 >> 1) * 8;
  const char* ob = (const char*)p.ows + ((size_t)((br * NB + b) * NH + h0) * 4 + w) * 12288 + lane * 16;
  f32x16 acc[5];
#pragma unroll
  for (int i = 0; i < 5; ++i) acc[i] = f32x16{};
  issue(0, 0); issue(1, 1);
  int buf = 0;
#pragma unroll 1
  for (int c = 0; c < 12; ++c) {
    bf16x8 of[4];
    const char* oc = ob + (size_t)(c / 3) * 4 * 12288 + (c % 3) * 4096;
#pragma unroll
    for (int s = 0; s < 4; ++s) of[s] = *(const bf16x8*)(oc + s * 1024);
    WAIT_BAR(0);
    const int nbuf = buf == 0 ? 2 : buf - 1;
    if (c + 2 < 12) issue(c + 2, nbuf);
    lds_cptr Lb = L + buf * 20480 + wo_rd;
#pragma unroll
    for (int s = 0; s < 4; ++s)
#pragma unroll
      for (int t = 0; t < 5; ++t) {
        lds_cptr a = Lb + t * 64 + 16 * s * 320;
        acc[t] = MFMA(cat(lds_tr(a), lds_tr(a + 4 * 320)), of[s], acc[t]);
      }
    buf = buf == 2 ? 0 : buf + 1;
  }
  float* pb = p.part + ((((size_t)(ks8 * 2 + br) * NB + b) * 4 + nt) * 4 + w) * (5 * 4 * 256) + lane * 4;
#pragma unroll
  for (int t = 0; t < 5; ++t)
#pragma unroll
    for (int g = 0; g < 4; ++g) *(f32x4*)(pb + (t * 4 + g) * 256) = f32x4{acc[t][4 * g], acc[t][4 * g + 1], acc[t][4 * g + 2], acc[t][4 * g + 3]};
}

__global__ __launch_bounds__(256) void k3_reduce(Params p) {
  const int g = blockIdx.x * 256 + threadIdx.x;
  const int lane = g & 63, u = g >> 6, s = u & 1, u2 = u >> 1, t = u2 % 5, u3 = u2 / 5, w = u3 & 3, nt = (u3 >> 2) & 3, b = (u3 >> 4) & 7, br = u3 >> 7;
  f32x4 a0 = {}, a1 = {};
#pragma unroll
  for (int ks = 0; ks < 8; ++ks) {
    const float* pb = p.part + ((((size_t)(ks * 2 + br) * NB + b) * 4 + nt) * 4 + w) * (5 * 4 * 256) + lane * 4 + (t * 4 + 2 * s) * 256;
    a0 += *(const f32x4*)pb; a1 += *(const f32x4*)(pb + 256);
  }
  const int q = 32 * w + (lane & 31), n = nt * 160 + 32 * t + 16 * s + 8 * (lane >> 5);
  u32x4 o = {cvtpk(a0[0], a0[1]), cvtpk(a0[2], a0[3]), cvtpk(a1[0], a1[1]), cvtpk(a1[2], a1[3])};
  *(u32x4*)((char*)p.out + ((size_t)(b * SQ + q) * 1280 + br * 640 + n) * 2) = o;
}

extern "C" void kernel_launch(void* const* d_in, const int* in_sizes, int n_in, void* d_out, int out_size, void* d_ws, size_t ws_size, hipStream_t stream) {
  Params p;
  for (int i = 0; i < 2; ++i) { p.x[i] = d_in[5 * i]; p.k[i] = d_in[5 * i + 1]; p.v[i] = d_in[5 * i + 2]; p.wq[i] = d_in[5 * i + 3]; p.wo[i] = d_in[5 * i + 4]; }
  p.ows = d_ws;
  p.part = (float*)((char*)d_ws + 25165824);
  p.out = d_out;
  hipFuncSetAttribute((const void*)k1_attn, hipFuncAttributeMaxDynamicSharedMemorySize, 81920);
  hipFuncSetAttribute((const void*)k2_oproj, hipFuncAttributeMaxDynamicSharedMemorySize, 61440);
  k1_attn<<<512, 256, 81920, stream>>>(p);
  k2_oproj<<<512, 256, 61440, stream>>>(p);
  k3_reduce<<<640, 256, 0, stream>>>(p);
}
```

```cpp
#include <hip/hip_runtime.h>
#include <hip/hip_bf16.h>
#include <stdint.h>
#include <type_traits>

typedef __attribute__((ext_vector_type(8))) short bf16x8;
typedef __attribute__((ext_vector_type(4))) short s16x4;
typedef __attribute__((ext_vector_type(16))) float f32x16;
typedef __attribute__((ext_vector_type(4))) float f32x4;
typedef __attribute__((ext_vector_type(4))) unsigned u32x4;
typedef __attribute__((address_space(3))) const char* lds_cptr;
typedef __attribute__((address_space(3))) char* lds_ptr;
#define DI __device__ __forceinline__
#define MFMA(a, b, c) __builtin_amdgcn_mfma_f32_32x32x16_bf16((a), (b), (c), 0, 0, 0)

constexpr int NB = 8, SQ = 128, DM = 640, NH = 32, SK = 512, DH = 192;
constexpr float QSCALE = 0.07216878364870322f * 1.4426950408889634f;
constexpr float THR = 8.f;

template <int I> using ic = std::integral_constant<int, I>;
template <int B, int E, class F> DI void sfor(F&& f) { if constexpr (B < E) { f(ic<B>{}); sfor<B + 1, E>(f); } }

DI unsigned cvtpk(float lo, float hi) { unsigned r; asm("v_cvt_pk_bf16_f32 %0, %1, %2" : "=v"(r) : "v"(lo), "v"(hi)); return r; }
DI bf16x8 pack8(const f32x16& x, int s, float m) {
  u32x4 w = {cvtpk(x[8 * s] * m, x[8 * s + 1] * m), cvtpk(x[8 * s + 2] * m, x[8 * s + 3] * m), cvtpk(x[8 * s + 4] * m, x[8 * s + 5] * m), cvtpk(x[8 * s + 6] * m, x[8 * s + 7] * m)};
  return __builtin_bit_cast(bf16x8, w);
}
DI bf16x8 lds_rd128(lds_cptr p) { return *(const __attribute__((address_space(3))) bf16x8*)p; }
DI s16x4 lds_tr(lds_cptr p) { return __builtin_bit_cast(s16x4, __builtin_amdgcn_ds_read_tr16_b64_v4i16((__attribute__((address_space(3))) s16x4*)p)); }
DI bf16x8 cat(s16x4 a, s16x4 b) { return __builtin_shufflevector(a, b, 0, 1, 2, 3, 4, 5, 6, 7); }
typedef __amdgpu_buffer_rsrc_t srd_t;
DI srd_t mksrd(const void* p, unsigned bytes) { return __builtin_amdgcn_make_buffer_rsrc((void*)p, (short)0, (int)bytes, 0x00020000); }
DI void dma16(unsigned ldsaddr, unsigned voff, srd_t srd, unsigned soff) {
  asm volatile("s_mov_b32 m0, %0\n\ts_nop 0\n\tbuffer_load_dwordx4 %1, %2, %3 offen lds" ::"s"(ldsaddr), "v"(voff), "s"(srd), "s"(soff) : "memory");
}
template <int N> DI void wait_bar() { asm volatile("s_waitcnt vmcnt(%0) lgkmcnt(0)\n\ts_barrier" ::"n"(N) : "memory"); }
DI unsigned rfl(unsigned x) { return __builtin_amdgcn_readfirstlane(x); }

struct Params {
  const void* x[2]; const void* k[2]; const void* v[2]; const void* wq[2]; const void* wo[2];
  void* ows; float* part; void* out;
};

constexpr int KV_K0_AT = 3, KV_V0_AT = 9;
constexpr int qcnt(int i) {
  if (i < 0) return 3;
  return (i + 2 < 20 ? 3 : 0) + (((i & 1) == 0 && i / 2 + 1 < 10) ? 2 : 0) + (i == KV_K0_AT ? 3 : 0) + (i == KV_V0_AT ? 3 : 0);
}

__global__ __launch_bounds__(512, 2) void k1_attn(Params p) {
  extern __shared__ __attribute__((aligned(16))) char smem[];
  const int tid = threadIdx.x, lane = tid & 63, r = lane & 31, hh = lane >> 5;
  const unsigned wv = rfl(tid >> 6), g = wv >> 2, w = wv & 3;
  const int bid = blockIdx.x, xg = bid & 7, idx = bid >> 3;
  const int br = xg >> 2, b = idx >> 2, h = (xg & 3) * 4 + (idx & 3) + 16 * g;
  const unsigned lds0 = (unsigned)(uintptr_t)smem;
  lds_cptr L = (lds_cptr)smem;
  const srd_t sx = mksrd((const char*)p.x[br] + (size_t)b * SQ * DM * 2, SQ * DM * 2);
  const srd_t swq = mksrd((const char*)p.wq[br] + h * DH * 2, DM * NH * DH * 2 - h * DH * 2);
  const srd_t sk = mksrd((const char*)p.k[br] + (size_t)(b * NH + h) * SK * DH * 2, SK * DH * 2);
  const srd_t sv = mksrd((const char*)p.v[br] + (size_t)(b * NH + h) * SK * DH * 2, SK * DH * 2);

  unsigned wq_off[3], x_off[2], k_off[3], v_off[3];
#pragma unroll
  for (int i = 0; i < 3; ++i) {
    const int pp = 64 * i + lane, row = pp / 24, pos = pp % 24;
    wq_off[i] = row * (NH * DH * 2) + (pos ^ (((row >> 1) & 1) << 2)) * 16;
    v_off[i] = row * 384 + (pos ^ (((row >> 1) & 1) << 2)) * 16;
    const int f = (4 * w + (row >> 1)) & 7;
    k_off[i] = row * 384 + ((pos & ~7) | ((pos & 7) ^ f)) * 16;
  }
#pragma unroll
  for (int i = 0; i < 2; ++i) x_off[i] = (lane >> 3) * (DM * 2) + (((lane & 7) ^ ((4 * i + (lane >> 4)) & 7)) * 16);

  constexpr unsigned WQ0 = 49152, X0 = 122880;
  auto issue_wq = [&](int i) {
#pragma unroll
    for (int j = 0; j < 3; ++j)
      dma16(lds0 + WQ0 + (i % 3) * 24576 + g * 12288 + w * 3072 + j * 1024, wq_off[j], swq, (unsigned)((i * 32 + 8 * w) * (NH * DH * 2)));
  };
  auto issue_x = [&](int c) {
#pragma unroll
    for (int j = 0; j < 2; ++j)
      dma16(lds0 + X0 + (c & 1) * 16384 + wv * 2048 + j * 1024, x_off[j], sx, (unsigned)((16 * wv + 8 * j) * (DM * 2) + c * 128));
  };
  auto issue_k = [&](int t, unsigned slot) {
#pragma unroll
    for (int i = 0; i < 3; ++i) dma16(lds0 + slot + w * 3072 + i * 1024, k_off[i], sk, (unsigned)(t * 12288 + w * 3072));
  };
  auto issue_v = [&](int t, unsigned slot) {
#pragma unroll
    for (int i = 0; i < 3; ++i) dma16(lds0 + slot + 12288 + w * 3072 + i * 1024, v_off[i], sv, (unsigned)(t * 12288 + w * 3072));
  };
  const unsigned slot0 = g * 24576, slot1 = 49152 + g * 49152, slot2 = 73728 + g * 49152;

  const int blk = (lane >> 4) & 1, q4 = (lane & 15) >> 2, p4 = lane & 3, bit = (q4 >> 1) & 1;
  const int trlo = blk * 32 + (p4 & 1) * 16 + (p4 >> 1) * 8;
  int wq_rd[2], x_rd[4];
#pragma unroll
  for (int v = 0; v < 2; ++v) wq_rd[v] = WQ0 + g * 12288 + (8 * hh + q4) * 384 + (v ^ bit) * 64 + trlo;
#pragma unroll
  for (int s = 0; s < 4; ++s) x_rd[s] = X0 + (32 * w + r) * 128 + (((2 * s + hh) ^ ((r >> 1) & 7)) * 16);

  f32x16 qa[6];
#pragma unroll
  for (int i = 0; i < 6; ++i) qa[i] = f32x16{};
  issue_wq(0); issue_x(0); issue_wq(1);
  sfor<0, 20>([&](auto I) {
    constexpr int i = decltype(I)::value;
    wait_bar<qcnt(i - 1)>();
    if constexpr (i + 2 < 20) issue_wq(i + 2);
    if constexpr ((i & 1) == 0 && i / 2 + 1 < 10) issue_x(i / 2 + 1);
    if constexpr (i == KV_K0_AT) issue_k(0, slot0);
    if constexpr (i == KV_V0_AT) issue_v(0, slot0);
#pragma unroll
    for (int ss = 0; ss < 2; ++ss) {
      const bf16x8 xb = lds_rd128(L + x_rd[2 * (i & 1) + ss] + ((i >> 1) & 1) * 16384);
#pragma unroll
      for (int dt = 0; dt < 6; ++dt) {
        lds_cptr a = L + wq_rd[dt & 1] + (i % 3) * 24576 + (dt >> 1) * 128 + 16 * ss * 384;
        const bf16x8 af = cat(lds_tr(a), lds_tr(a + 4 * 384));
        qa[dt] = MFMA(af, xb, qa[dt]);
      }
    }
  });
  bf16x8 qf[12];
#pragma unroll
  for (int dt = 0; dt < 6; ++dt) { qf[2 * dt] = pack8(qa[dt], 0, QSCALE); qf[2 * dt + 1] = pack8(qa[dt], 1, QSCALE); }

  int k_rd[4], v_rd[2];
#pragma unroll
  for (int s = 0; s < 4; ++s) k_rd[s] = r * 384 + (((2 * s + hh) ^ ((r >> 1) & 7)) * 16);
#pragma unroll
  for (int v = 0; v < 2; ++v) v_rd[v] = 12288 + (4 * hh + q4) * 384 + (v ^ bit) * 64 + trlo;

  wait_bar<0>();
  issue_k(1, slot1); issue_v(1, slot1);
  f32x16 o[6];
#pragma unroll
  for (int i = 0; i < 6; ++i) o[i] = f32x16{};
  float m = 0.f, l = 0.f;
  unsigned scur = slot0, snxt = slot1, snn = slot2;
#pragma unroll 1
  for (int t = 0; t < 16; ++t) {
    if (t + 1 < 16) wait_bar<6>(); else wait_bar<0>();
    if (t + 2 < 16) { issue_k(t + 2, snn); issue_v(t + 2, snn); }
    lds_cptr Lk = L + scur;
    f32x16 s = f32x16{};
#pragma unroll
    for (int ks = 0; ks < 12; ++ks) s = MFMA(lds_rd128(Lk + k_rd[ks & 3] + (ks >> 2) * 128), qf[ks], s);
    float mx = fmaxf(fmaxf(s[0], s[1]), s[2]);
#pragma unroll
    for (int i = 3; i < 15; i += 2) mx = fmaxf(fmaxf(mx, s[i]), s[i + 1]);
    mx = fmaxf(mx, s[15]);
    { auto rr = __builtin_amdgcn_permlane32_swap(__float_as_uint(mx), __float_as_uint(mx), false, false);
      mx = fmaxf(__uint_as_float(rr[0]), __uint_as_float(rr[1])); }
    if (t == 0) { m = mx; }
    else if (__builtin_expect(__any(mx - m > THR), 0)) {
      const float mn = fmaxf(m, mx), f = __builtin_amdgcn_exp2f(m - mn);
      m = mn; l *= f;
#pragma unroll
      for (int d = 0; d < 6; ++d)
#pragma unroll
        for (int i = 0; i < 16; ++i) o[d][i] *= f;
    }
    float ps = 0.f;
#pragma unroll
    for (int i = 0; i < 16; ++i) { s[i] = __builtin_amdgcn_exp2f(s[i] - m); ps += s[i]; }
    l += ps;
    const bf16x8 pf0 = pack8(s, 0, 1.f), pf1 = pack8(s, 1, 1.f);
#pragma unroll
    for (int dt = 0; dt < 6; ++dt) {
      lds_cptr a = Lk + v_rd[dt & 1] + (dt >> 1) * 128;
      o[dt] = MFMA(cat(lds_tr(a), lds_tr(a + 8 * 384)), pf0, o[dt]);
      o[dt] = MFMA(cat(lds_tr(a + 16 * 384), lds_tr(a + 24 * 384)), pf1, o[dt]);
    }
    const unsigned tmp = scur; scur = snxt; snxt = snn; snn = tmp;
  }
  { auto rr = __builtin_amdgcn_permlane32_swap(__float_as_uint(l), __float_as_uint(l), false, false);
    l = __uint_as_float(rr[0]) + __uint_as_float(rr[1]); }
  const float inv = 1.0f / l;
  char* ob = (char*)p.ows + ((size_t)((br * NB + b) * NH + h) * 4 + w) * 12288 + lane * 16;
#pragma unroll
  for (int dt = 0; dt < 6; ++dt) {
    *(bf16x8*)(ob + (2 * dt) * 1024) = pack8(o[dt], 0, inv);
    *(bf16x8*)(ob + (2 * dt + 1) * 1024) = pack8(o[dt], 1, inv);
  }
}

DI void gload16(bf16x8& d, const void* base, unsigned voff) {
  asm volatile("global_load_dwordx4 %0, %1, %2" : "=v"(d) : "v"(voff), "s"(base) : "memory");
}
__global__ __launch_bounds__(512, 2) void k2_oproj(Params p) {
  extern __shared__ __attribute__((aligned(16))) char smem[];
  const int tid = threadIdx.x, lane = tid & 63, hh = lane >> 5;
  const unsigned wv = rfl(tid >> 6), g = wv >> 2, w = wv & 3;
  const int bid = blockIdx.x, xg = bid & 7, idx = bid >> 3;
  const int br = xg >> 2, hq = xg & 3, b = idx >> 2, nt = idx & 3;
  const int h0 = hq * 4 + 16 * g;
  const unsigned lds0 = (unsigned)(uintptr_t)smem;
  lds_cptr L = (lds_cptr)smem;
  const srd_t swo = mksrd((const char*)p.wo[br] + (size_t)h0 * DH * DM * 2 + nt * 320, 768 * DM * 2 - nt * 320);
  unsigned wo_off[5];
#pragma unroll
  for (int i = 0; i < 5; ++i) { const int pp = 64 * i + lane; wo_off[i] = (pp / 20) * (DM * 2) + (pp % 20) * 16; }
  const char* ob = (const char*)p.ows + ((size_t)((br * NB + b) * NH + h0) * 4 + w) * 12288;
  const unsigned ovo = lane * 16;
  bf16x8 of[3][4];
  auto issue = [&](auto C) {
    constexpr int c = decltype(C)::value;
#pragma unroll
    for (int i = 0; i < 5; ++i) dma16(lds0 + (c % 3) * 40960 + g * 20480 + w * 5120 + i * 1024, wo_off[i], swo, (unsigned)((c * 64 + 16 * w) * (DM * 2)));
    const char* oc = ob + (size_t)(c / 3) * 4 * 12288 + (c % 3) * 4096;
#pragma unroll
    for (int s = 0; s < 4; ++s) gload16(of[c % 3][s], oc + s * 1024, ovo);
  };
  const int blk = (lane >> 4) & 1, q4 = (lane & 15) >> 2, p4 = lane & 3;
  const int wo_rd = g * 20480 + (8 * hh + q4) * 320 + blk * 32 + (p4 & 1) * 16 + (p4 >> 1) * 8;
  f32x16 acc[5];
#pragma unroll
  for (int i = 0; i < 5; ++i) acc[i] = f32x16{};
  issue(ic<0>{}); issue(ic<1>{});
  sfor<0, 12>([&](auto C) {
    constexpr int c = decltype(C)::value;
    asm volatile("s_waitcnt vmcnt(%4) lgkmcnt(0)\n\ts_barrier" : "+v"(of[c % 3][0]), "+v"(of[c % 3][1]), "+v"(of[c % 3][2]), "+v"(of[c % 3][3]) : "n"(c + 1 < 12 ? 9 : 0) : "memory");
    if constexpr (c + 2 < 12) issue(ic<c + 2>{});
    lds_cptr Lb = L + (c % 3) * 40960 + wo_rd;
#pragma unroll
    for (int s = 0; s < 4; ++s)
#pragma unroll
      for (int t = 0; t < 5; ++t) {
        lds_cptr a = Lb + t * 64 + 16 * s * 320;
        acc[t] = MFMA(cat(lds_tr(a), lds_tr(a + 4 * 320)), of[c % 3][s], acc[t]);
      }
  });
  wait_bar<0>();
  lds_ptr LW = (lds_ptr)smem;
  typedef __attribute__((address_space(3))) f32x4* lds_f4;
  auto q4of = [&](int ht, int u) { const f32x16& a = acc[ht >> 1]; const int o = (ht & 1) * 8 + 4 * u; return f32x4{a[o], a[o + 1], a[o + 2], a[o + 3]}; };
  if (g == 0) {
#pragma unroll
    for (int j = 0; j < 5; ++j)
#pragma unroll
      for (int u = 0; u < 2; ++u) *(lds_f4)(LW + wv * 10240 + (2 * j + u) * 1024 + lane * 16) = q4of(5 + j, u);
  } else {
#pragma unroll
    for (int j = 0; j < 5; ++j)
#pragma unroll
      for (int u = 0; u < 2; ++u) *(lds_f4)(LW + wv * 10240 + (2 * j + u) * 1024 + lane * 16) = q4of(j, u);
  }
  wait_bar<0>();
  float* pb = p.part + ((((size_t)(hq * 2 + br) * NB + b) * 4 + nt) * 4 + w) * (20 * 256) + lane * 4;
  const unsigned other = (wv ^ 4) * 10240;
  if (g == 0) {
#pragma unroll
    for (int j = 0; j < 5; ++j)
#pragma unroll
      for (int u = 0; u < 2; ++u) *(f32x4*)(pb + (2 * j + u) * 256) = q4of(j, u) + *(lds_f4)(LW + other + (2 * j + u) * 1024 + lane * 16);
  } else {
#pragma unroll
    for (int j = 0; j < 5; ++j)
#pragma unroll
      for (int u = 0; u < 2; ++u) *(f32x4*)(pb + (2 * (5 + j) + u) * 256) = q4of(5 + j, u) + *(lds_f4)(LW + other + (2 * j + u) * 1024 + lane * 16);
  }
}

__global__ __launch_bounds__(256) void k3_reduce(Params p) {
  const int gi = blockIdx.x * 256 + threadIdx.x;
  const int lane = gi & 63, u = gi >> 6, ht = u % 10, u3 = u / 10, w = u3 & 3, nt = (u3 >> 2) & 3, b = (u3 >> 4) & 7, br = u3 >> 7;
  f32x4 a0 = {}, a1 = {};
#pragma unroll
  for (int ks = 0; ks < 4; ++ks) {
    const float* pb = p.part + ((((size_t)(ks * 2 + br) * NB + b) * 4 + nt) * 4 + w) * (20 * 256) + lane * 4 + ht * 512;
    a0 += *(const f32x4*)pb; a1 += *(const f32x4*)(pb + 256);
  }
  const int q = 32 * w + (lane & 31), n = nt * 160 + 16 * ht + 8 * (lane >> 5);
  u32x4 o = {cvtpk(a0[0], a0[1]), cvtpk(a0[2], a0[3]), cvtpk(a1[0], a1[1]), cvtpk(a1[2], a1[3])};
  *(u32x4*)((char*)p.out + ((size_t)(b * SQ + q) * 1280 + br * 640 + n) * 2) = o;
}

extern "C" void kernel_launch(void* const* d_in, const int* in_sizes, int n_in, void* d_out, int out_size, void* d_ws, size_t ws_size, hipStream_t stream) {
  Params p;
  for (int i = 0; i < 2; ++i) { p.x[i] = d_in[5 * i]; p.k[i] = d_in[5 * i + 1]; p.v[i] = d_in[5 * i + 2]; p.wq[i] = d_in[5 * i + 3]; p.wo[i] = d_in[5 * i + 4]; }
  p.ows = d_ws;
  p.part = (float*)((char*)d_ws + 25165824);
  p.out = d_out;
  hipFuncSetAttribute((const void*)k1_attn, hipFuncAttributeMaxDynamicSharedMemorySize, 155648);
  hipFuncSetAttribute((const void*)k2_oproj, hipFuncAttributeMaxDynamicSharedMemorySize, 122880);
  k1_attn<<<256, 512, 155648, stream>>>(p);
  k2_oproj<<<256, 512, 122880, stream>>>(p);
  k3_reduce<<<640, 256, 0, stream>>>(p);
}
```

```cpp
#include <hip/hip_runtime.h>
#include <hip/hip_bf16.h>
#include <stdint.h>
#include <type_traits>

typedef __attribute__((ext_vector_type(8))) short bf16x8;
typedef __attribute__((ext_vector_type(4))) short s16x4;
typedef __attribute__((ext_vector_type(16))) float f32x16;
typedef __attribute__((ext_vector_type(4))) float f32x4;
typedef __attribute__((ext_vector_type(4))) unsigned u32x4;
typedef __attribute__((address_space(3))) const char* lds_cptr;
typedef __attribute__((address_space(3))) char* lds_ptr;
#define DI __device__ __forceinline__
#define MFMA(a, b, c) __builtin_amdgcn_mfma_f32_32x32x16_bf16((a), (b), (c), 0, 0, 0)

constexpr int NB = 8, SQ = 128, DM = 640, NH = 32, SK = 512, DH = 192;
constexpr float QSCALE = 0.07216878364870322f * 1.4426950408889634f;
constexpr float THR = 8.f;

template <int I> using ic = std::integral_constant<int, I>;
template <int B, int E, class F> DI void sfor(F&& f) { if constexpr (B < E) { f(ic<B>{}); sfor<B + 1, E>(f); } }

DI unsigned cvtpk(float lo, float hi) { unsigned r; asm("v_cvt_pk_bf16_f32 %0, %1, %2" : "=v"(r) : "v"(lo), "v"(hi)); return r; }
DI bf16x8 pack8(const f32x16& x, int s, float m) {
  u32x4 w = {cvtpk(x[8 * s] * m, x[8 * s + 1] * m), cvtpk(x[8 * s + 2] * m, x[8 * s + 3] * m), cvtpk(x[8 * s + 4] * m, x[8 * s + 5] * m), cvtpk(x[8 * s + 6] * m, x[8 * s + 7] * m)};
  return __builtin_bit_cast(bf16x8, w);
}
DI bf16x8 lds_rd128(lds_cptr p) { return *(const __attribute__((address_space(3))) bf16x8*)p; }
DI s16x4 lds_tr(lds_cptr p) { return __builtin_bit_cast(s16x4, __builtin_amdgcn_ds_read_tr16_b64_v4i16((__attribute__((address_space(3))) s16x4*)p)); }
DI bf16x8 cat(s16x4 a, s16x4 b) { return __builtin_shufflevector(a, b, 0, 1, 2, 3, 4, 5, 6, 7); }
typedef __amdgpu_buffer_rsrc_t srd_t;
DI srd_t mksrd(const void* p, unsigned bytes) { return __builtin_amdgcn_make_buffer_rsrc((void*)p, (short)0, (int)bytes, 0x00020000); }
DI void dma16(unsigned ldsaddr, unsigned voff, srd_t srd, unsigned soff) {
  asm volatile("s_mov_b32 m0, %0\n\ts_nop 0\n\tbuffer_load_dwordx4 %1, %2, %3 offen lds" ::"s"(ldsaddr), "v"(voff), "s"(srd), "s"(soff) : "memory");
}
template <int N> DI void wait_bar() { asm volatile("s_waitcnt vmcnt(%0) lgkmcnt(0)\n\ts_barrier" ::"n"(N) : "memory"); }
DI unsigned rfl(unsigned x) { return __builtin_amdgcn_readfirstlane(x); }

#define STAMP(kid, k)
struct Params {
  const void* x[2]; const void* k[2]; const void* v[2]; const void* wq[2]; const void* wo[2];
  void* ows; float* part; void* out;
};

constexpr int KV_K0_AT = 3, KV_V0_AT = 9;
constexpr int qcnt(int i) {
  if (i < 0) return 3;
  return (i + 2 < 20 ? 3 : 0) + (((i & 1) == 0 && i / 2 + 1 < 10) ? 2 : 0) + (i == KV_K0_AT ? 3 : 0) + (i == KV_V0_AT ? 3 : 0);
}

__global__ __launch_bounds__(512, 2) void k1_attn(Params p) {
  extern __shared__ __attribute__((aligned(16))) char smem[];
  const int tid = threadIdx.x, lane = tid & 63, r = lane & 31, hh = lane >> 5;
  const unsigned wv = rfl(tid >> 6), g = wv >> 2, w = wv & 3;
  const int bid = blockIdx.x, xg = bid & 7, idx = bid >> 3;
  const int br = xg >> 2, b = idx >> 2, h = (xg & 3) * 4 + (idx & 3) + 16 * g;
  STAMP(0, 0);
  const unsigned lds0 = (unsigned)(uintptr_t)smem;
  lds_cptr L = (lds_cptr)smem;
  const srd_t sx = mksrd((const char*)p.x[br] + (size_t)b * SQ * DM * 2, SQ * DM * 2);
  const srd_t swq = mksrd((const char*)p.wq[br] + h * DH * 2, DM * NH * DH * 2 - h * DH * 2);
  const srd_t sk = mksrd((const char*)p.k[br] + (size_t)(b * NH + h) * SK * DH * 2, SK * DH * 2);
  const srd_t sv = mksrd((const char*)p.v[br] + (size_t)(b * NH + h) * SK * DH * 2, SK * DH * 2);

  unsigned wq_off[3], x_off[2], k_off[3], v_off[3];
#pragma unroll
  for (int i = 0; i < 3; ++i) {
    const int pp = 64 * i + lane, row = pp / 24, pos = pp % 24;
    wq_off[i] = row * (NH * DH * 2) + (pos ^ (((row >> 1) & 1) << 2)) * 16;
    v_off[i] = row * 384 + (pos ^ (((row >> 1) & 1) << 2)) * 16;
    const int f = (4 * w + (row >> 1)) & 7;
    k_off[i] = row * 384 + ((pos & ~7) | ((pos & 7) ^ f)) * 16;
  }
#pragma unroll
  for (int i = 0; i < 2; ++i) x_off[i] = (lane >> 3) * (DM * 2) + (((lane & 7) ^ ((4 * i + (lane >> 4)) & 7)) * 16);

  constexpr unsigned WQ0 = 49152, X0 = 122880;
  auto issue_wq = [&](int i) {
#pragma unroll
    for (int j = 0; j < 3; ++j)
      dma16(lds0 + WQ0 + (i % 3) * 24576 + g * 12288 + w * 3072 + j * 1024, wq_off[j], swq, (unsigned)((i * 32 + 8 * w) * (NH * DH * 2)));
  };
  auto issue_x = [&](int c) {
#pragma unroll
    for (int j = 0; j < 2; ++j)
      dma16(lds0 + X0 + (c & 1) * 16384 + wv * 2048 + j * 1024, x_off[j], sx, (unsigned)((16 * wv + 8 * j) * (DM * 2) + c * 128));
  };
  auto issue_k = [&](int t, unsigned slot) {
#pragma unroll
    for (int i = 0; i < 3; ++i) dma16(lds0 + slot + w * 3072 + i * 1024, k_off[i], sk, (unsigned)(t * 12288 + w * 3072));
  };
  auto issue_v = [&](int t, unsigned slot) {
#pragma unroll
    for (int i = 0; i < 3; ++i) dma16(lds0 + slot + 12288 + w * 3072 + i * 1024, v_off[i], sv, (unsigned)(t * 12288 + w * 3072));
  };
  const unsigned slot0 = g * 24576, slot1 = 49152 + g * 49152, slot2 = 73728 + g * 49152;

  const int blk = (lane >> 4) & 1, q4 = (lane & 15) >> 2, p4 = lane & 3, bit = (q4 >> 1) & 1;
  const int trlo = blk * 32 + (p4 & 1) * 16 + (p4 >> 1) * 8;
  int wq_rd[2], x_rd[4];
#pragma unroll
  for (int v = 0; v < 2; ++v) wq_rd[v] = WQ0 + g * 12288 + (8 * hh + q4) * 384 + (v ^ bit) * 64 + trlo;
#pragma unroll
  for (int s = 0; s < 4; ++s) x_rd[s] = X0 + (32 * w + r) * 128 + (((2 * s + hh) ^ ((r >> 1) & 7)) * 16);

  f32x16 qa[6];
#pragma unroll
  for (int i = 0; i < 6; ++i) qa[i] = f32x16{};
  issue_wq(0); issue_x(0); issue_wq(1);
  sfor<0, 20>([&](auto I) {
    constexpr int i = decltype(I)::value;
    wait_bar<qcnt(i - 1)>();
    if constexpr (i + 2 < 20) issue_wq(i + 2);
    if constexpr ((i & 1) == 0 && i / 2 + 1 < 10) issue_x(i / 2 + 1);
    if constexpr (i == KV_K0_AT) issue_k(0, slot0);
    if constexpr (i == KV_V0_AT) issue_v(0, slot0);
#pragma unroll
    for (int ss = 0; ss < 2; ++ss) {
      const bf16x8 xb = lds_rd128(L + x_rd[2 * (i & 1) + ss] + ((i >> 1) & 1) * 16384);
#pragma unroll
      for (int dt = 0; dt < 6; ++dt) {
        lds_cptr a = L + wq_rd[dt & 1] + (i % 3) * 24576 + (dt >> 1) * 128 + 16 * ss * 384;
        const bf16x8 af = cat(lds_tr(a), lds_tr(a + 4 * 384));
        qa[dt] = MFMA(af, xb, qa[dt]);
      }
    }
  });
  bf16x8 qf[12];
#pragma unroll
  for (int dt = 0; dt < 6; ++dt) { qf[2 * dt] = pack8(qa[dt], 0, QSCALE); qf[2 * dt + 1] = pack8(qa[dt], 1, QSCALE); }

  int k_rd[4], v_rd[2];
#pragma unroll
  for (int s = 0; s < 4; ++s) k_rd[s] = r * 384 + (((2 * s + hh) ^ ((r >> 1) & 7)) * 16);
#pragma unroll
  for (int v = 0; v < 2; ++v) v_rd[v] = 12288 + (4 * hh + q4) * 384 + (v ^ bit) * 64 + trlo;

  wait_bar<0>();
  STAMP(0, 1);
  issue_k(1, slot1); issue_v(1, slot1);
  f32x16 o[6];
#pragma unroll
  for (int i = 0; i < 6; ++i) o[i] = f32x16{};
  float m = 0.f, l = 0.f;
  unsigned scur = slot0, snxt = slot1, snn = slot2;
#pragma unroll 1
  for (int t = 0; t < 16; ++t) {
    if (t + 1 < 16) wait_bar<6>(); else wait_bar<0>();
    if (t + 2 < 16) { issue_k(t + 2, snn); issue_v(t + 2, snn); }
    lds_cptr Lk = L + scur;
    f32x16 s = f32x16{};
#pragma unroll
    for (int ks = 0; ks < 12; ++ks) s = MFMA(lds_rd128(Lk + k_rd[ks & 3] + (ks >> 2) * 128), qf[ks], s);
    float mx = fmaxf(fmaxf(s[0], s[1]), s[2]);
#pragma unroll
    for (int i = 3; i < 15; i += 2) mx = fmaxf(fmaxf(mx, s[i]), s[i + 1]);
    mx = fmaxf(mx, s[15]);
    { auto rr = __builtin_amdgcn_permlane32_swap(__float_as_uint(mx), __float_as_uint(mx), false, false);
      mx = fmaxf(__uint_as_float(rr[0]), __uint_as_float(rr[1])); }
    if (t == 0) { m = mx; }
    else if (__builtin_expect(__any(mx - m > THR), 0)) {
      const float mn = fmaxf(m, mx), f = __builtin_amdgcn_exp2f(m - mn);
      m = mn; l *= f;
#pragma unroll
      for (int d = 0; d < 6; ++d)
#pragma unroll
        for (int i = 0; i < 16; ++i) o[d][i] *= f;
    }
    float ps = 0.f;
#pragma unroll
    for (int i = 0; i < 16; ++i) { s[i] = __builtin_amdgcn_exp2f(s[i] - m); ps += s[i]; }
    l += ps;
    const bf16x8 pf0 = pack8(s, 0, 1.f), pf1 = pack8(s, 1, 1.f);
#pragma unroll
    for (int dt = 0; dt < 6; ++dt) {
      lds_cptr a = Lk + v_rd[dt & 1] + (dt >> 1) * 128;
      o[dt] = MFMA(cat(lds_tr(a), lds_tr(a + 8 * 384)), pf0, o[dt]);
      o[dt] = MFMA(cat(lds_tr(a + 16 * 384), lds_tr(a + 24 * 384)), pf1, o[dt]);
    }
    const unsigned tmp = scur; scur = snxt; snxt = snn; snn = tmp;
  }
  STAMP(0, 2);
  { auto rr = __builtin_amdgcn_permlane32_swap(__float_as_uint(l), __float_as_uint(l), false, false);
    l = __uint_as_float(rr[0]) + __uint_as_float(rr[1]); }
  const float inv = 1.0f / l;
  char* ob = (char*)p.ows + ((size_t)((br * NB + b) * NH + h) * 4 + w) * 12288 + lane * 16;
#pragma unroll
  for (int dt = 0; dt < 6; ++dt) {
    *(bf16x8*)(ob + (2 * dt) * 1024) = pack8(o[dt], 0, inv);
    *(bf16x8*)(ob + (2 * dt + 1) * 1024) = pack8(o[dt], 1, inv);
  }
  STAMP(0, 3);
}

DI void gload16(bf16x8& d, const void* base, unsigned voff) {
  asm volatile("global_load_dwordx4 %0, %1, %2" : "=v"(d) : "v"(voff), "s"(base) : "memory");
}
__global__ __launch_bounds__(512, 2) void k2_oproj(Params p) {
  extern __shared__ __attribute__((aligned(16))) char smem[];
  const int tid = threadIdx.x, lane = tid & 63, hh = lane >> 5;
  const unsigned wv = rfl(tid >> 6), g = wv >> 2, w = wv & 3;
  const int bid = blockIdx.x, xg = bid & 7, idx = bid >> 3;
  const int br = xg >> 2, hq = xg & 3, b = idx >> 2, nt = idx & 3;
  const int h0 = hq * 4 + 16 * g;
  STAMP(1, 0);
  const unsigned lds0 = (unsigned)(uintptr_t)smem;
  lds_cptr L = (lds_cptr)smem;
  const srd_t swo = mksrd((const char*)p.wo[br] + (size_t)h0 * DH * DM * 2 + nt * 320, 768 * DM * 2 - nt * 320);
  unsigned wo_off[5];
#pragma unroll
  for (int i = 0; i < 5; ++i) { const int pp = 64 * i + lane; wo_off[i] = (pp / 20) * (DM * 2) + (pp % 20) * 16; }
  const char* ob = (const char*)p.ows + ((size_t)((br * NB + b) * NH + h0) * 4 + w) * 12288;
  const unsigned ovo = lane * 16;
  bf16x8 of[3][4];
  auto issue = [&](auto C) {
    constexpr int c = decltype(C)::value;
#pragma unroll
    for (int i = 0; i < 5; ++i) dma16(lds0 + (c % 3) * 40960 + g * 20480 + w * 5120 + i * 1024, wo_off[i], swo, (unsigned)((c * 64 + 16 * w) * (DM * 2)));
    const char* oc = ob + (size_t)(c / 3) * 4 * 12288 + (c % 3) * 4096;
#pragma unroll
    for (int s = 0; s < 4; ++s) gload16(of[c % 3][s], oc + s * 1024, ovo);
  };
  const int blk = (lane >> 4) & 1, q4 = (lane & 15) >> 2, p4 = lane & 3;
  const int wo_rd = g * 20480 + (8 * hh + q4) * 320 + blk * 32 + (p4 & 1) * 16 + (p4 >> 1) * 8;
  f32x16 acc[5];
#pragma unroll
  for (int i = 0; i < 5; ++i) acc[i] = f32x16{};
  issue(ic<0>{}); issue(ic<1>{});
  sfor<0, 12>([&](auto C) {
    constexpr int c = decltype(C)::value;
    asm volatile("s_waitcnt vmcnt(%4) lgkmcnt(0)\n\ts_barrier" : "+v"(of[c % 3][0]), "+v"(of[c % 3][1]), "+v"(of[c % 3][2]), "+v"(of[c % 3][3]) : "n"(c + 1 < 12 ? 9 : 0) : "memory");
    if constexpr (c + 2 < 12) issue(ic<c + 2>{});
    lds_cptr Lb = L + (c % 3) * 40960 + wo_rd;
#pragma unroll
    for (int s = 0; s < 4; ++s)
#pragma unroll
      for (int t = 0; t < 5; ++t) {
        lds_cptr a = Lb + t * 64 + 16 * s * 320;
        acc[t] = MFMA(cat(lds_tr(a), lds_tr(a + 4 * 320)), of[c % 3][s], acc[t]);
      }
  });
  wait_bar<0>();
  STAMP(1, 1);
  lds_ptr LW = (lds_ptr)smem;
  typedef __attribute__((address_space(3))) f32x4* lds_f4;
  auto q4of = [&](int ht, int u) { const f32x16& a = acc[ht >> 1]; const int o = (ht & 1) * 8 + 4 * u; return f32x4{a[o], a[o + 1], a[o + 2], a[o + 3]}; };
  if (g == 0) {
#pragma unroll
    for (int j = 0; j < 5; ++j)
#pragma unroll
      for (int u = 0; u < 2; ++u) *(lds_f4)(LW + wv * 10240 + (2 * j + u) * 1024 + lane * 16) = q4of(5 + j, u);
  } else {
#pragma unroll
    for (int j = 0; j < 5; ++j)
#pragma unroll
      for (int u = 0; u < 2; ++u) *(lds_f4)(LW + wv * 10240 + (2 * j + u) * 1024 + lane * 16) = q4of(j, u);
  }
  wait_bar<0>();
  float* pb = p.part + ((((size_t)(hq * 2 + br) * NB + b) * 4 + nt) * 4 + w) * (20 * 256) + lane * 4;
  const unsigned other = (wv ^ 4) * 10240;
  if (g == 0) {
#pragma unroll
    for (int j = 0; j < 5; ++j)
#pragma unroll
      for (int u = 0; u < 2; ++u) *(f32x4*)(pb + (2 * j + u) * 256) = q4of(j, u) + *(lds_f4)(LW + other + (2 * j + u) * 1024 + lane * 16);
  } else {
#pragma unroll
    for (int j = 0; j < 5; ++j)
#pragma unroll
      for (int u = 0; u < 2; ++u) *(f32x4*)(pb + (2 * (5 + j) + u) * 256) = q4of(5 + j, u) + *(lds_f4)(LW + other + (2 * j + u) * 1024 + lane * 16);
  }
  STAMP(1, 2);
  STAMP(1, 3);
}

__global__ __launch_bounds__(256) void k3_reduce(Params p) {
  const int gi = blockIdx.x * 256 + threadIdx.x;
  const int lane = gi & 63, u = gi >> 6, ht = u % 10, u3 = u / 10, w = u3 & 3, nt = (u3 >> 2) & 3, b = (u3 >> 4) & 7, br = u3 >> 7;
  STAMP(2, 0);
  f32x4 a0 = {}, a1 = {};
#pragma unroll
  for (int ks = 0; ks < 4; ++ks) {
    const float* pb = p.part + ((((size_t)(ks * 2 + br) * NB + b) * 4 + nt) * 4 + w) * (20 * 256) + lane * 4 + ht * 512;
    a0 += *(const f32x4*)pb; a1 += *(const f32x4*)(pb + 256);
  }
  const int q = 32 * w + (lane & 31), n = nt * 160 + 16 * ht + 8 * (lane >> 5);
  u32x4 o = {cvtpk(a0[0], a0[1]), cvtpk(a0[2], a0[3]), cvtpk(a1[0], a1[1]), cvtpk(a1[2], a1[3])};
  *(u32x4*)((char*)p.out + ((size_t)(b * SQ + q) * 1280 + br * 640 + n) * 2) = o;
  STAMP(2, 1); STAMP(2, 2); STAMP(2, 3);
}

extern "C" void kernel_launch(void* const* d_in, const int* in_sizes, int n_in, void* d_out, int out_size, void* d_ws, size_t ws_size, hipStream_t stream) {
  Params p;
  for (int i = 0; i < 2; ++i) { p.x[i] = d_in[5 * i]; p.k[i] = d_in[5 * i + 1]; p.v[i] = d_in[5 * i + 2]; p.wq[i] = d_in[5 * i + 3]; p.wo[i] = d_in[5 * i + 4]; }
  p.ows = d_ws;
  p.part = (float*)((char*)d_ws + 25165824);
  p.out = d_out;
  hipFuncSetAttribute((const void*)k1_attn, hipFuncAttributeMaxDynamicSharedMemorySize, 155648);
  hipFuncSetAttribute((const void*)k2_oproj, hipFuncAttributeMaxDynamicSharedMemorySize, 122880);
  k1_attn<<<256, 512, 155648, stream>>>(p);
  k2_oproj<<<256, 512, 122880, stream>>>(p);
  k3_reduce<<<640, 256, 0, stream>>>(p);
}
```

```cpp
#include <hip/hip_runtime.h>
#include <hip/hip_bf16.h>
#include <stdint.h>
#include <type_traits>

typedef __attribute__((ext_vector_type(8))) short bf16x8;
typedef __attribute__((ext_vector_type(4))) short s16x4;
typedef __attribute__((ext_vector_type(16))) float f32x16;
typedef __attribute__((ext_vector_type(4))) float f32x4;
typedef __attribute__((ext_vector_type(4))) unsigned u32x4;
typedef __attribute__((address_space(3))) const char* lds_cptr;
typedef __attribute__((address_space(3))) char* lds_ptr;
typedef __attribute__((address_space(3))) bf16x8* lds_b8;
typedef __attribute__((address_space(1))) unsigned gu32;
#define DI __device__ __forceinline__
#define AI __attribute__((always_inline))
#define MFMA(a, b, c) __builtin_amdgcn_mfma_f32_32x32x16_bf16((a), (b), (c), 0, 0, 0)
#define SB() __builtin_amdgcn_sched_barrier(0)

constexpr int NB = 8, SQ = 128, DM = 640, NH = 32, SK = 512, DH = 192;
constexpr float QSCALE = 0.07216878364870322f * 1.4426950408889634f;
constexpr float THR = 8.f / QSCALE;

template <int I> using ic = std::integral_constant<int, I>;
template <int B, int E, class F> DI void sfor(F&& f) { if constexpr (B < E) { f(ic<B>{}); sfor<B + 1, E>(f); } }

typedef __attribute__((ext_vector_type(2))) float f32x2;
typedef __attribute__((ext_vector_type(2))) __bf16 bf16x2;
DI unsigned cvtpk(float lo, float hi) { return __builtin_bit_cast(unsigned, __builtin_convertvector(f32x2{lo, hi}, bf16x2)); }
DI bf16x8 pack8(const f32x16& x, int s, float m) {
  u32x4 w = {cvtpk(x[8 * s] * m, x[8 * s + 1] * m), cvtpk(x[8 * s + 2] * m, x[8 * s + 3] * m), cvtpk(x[8 * s + 4] * m, x[8 * s + 5] * m), cvtpk(x[8 * s + 6] * m, x[8 * s + 7] * m)};
  return __builtin_bit_cast(bf16x8, w);
}
DI bf16x8 pack8(const f32x16& x, int s) {
  u32x4 w = {cvtpk(x[8 * s], x[8 * s + 1]), cvtpk(x[8 * s + 2], x[8 * s + 3]), cvtpk(x[8 * s + 4], x[8 * s + 5]), cvtpk(x[8 * s + 6], x[8 * s + 7])};
  return __builtin_bit_cast(bf16x8, w);
}
DI bf16x8 lds_rd128(lds_cptr p) { return *(const __attribute__((address_space(3))) bf16x8*)p; }
DI s16x4 lds_tr(lds_cptr p) { return __builtin_bit_cast(s16x4, __builtin_amdgcn_ds_read_tr16_b64_v4i16((__attribute__((address_space(3))) s16x4*)p)); }
DI bf16x8 cat(s16x4 a, s16x4 b) { return __builtin_shufflevector(a, b, 0, 1, 2, 3, 4, 5, 6, 7); }
typedef __amdgpu_buffer_rsrc_t srd_t;
DI srd_t mksrd(const void* p, unsigned bytes) { return __builtin_amdgcn_make_buffer_rsrc((void*)p, (short)0, (int)bytes, 0x00020000); }
template <int I, int NT = 0> DI void dma_piece(unsigned ldsaddr, unsigned voff, srd_t srd, unsigned soff) {
  if constexpr (NT) {
    if constexpr (I == 0) asm volatile("s_mov_b32 m0, %0\n\ts_nop 0\n\tbuffer_load_dwordx4 %1, %2, %3 offen nt lds" ::"s"(ldsaddr), "v"(voff), "s"(srd), "s"(soff));
    else asm volatile("buffer_load_dwordx4 %0, %1, %2 offen offset:%3 nt lds" ::"v"(voff), "s"(srd), "s"(soff), "n"(I * 1024));
  } else {
    if constexpr (I == 0) asm volatile("s_mov_b32 m0, %0\n\ts_nop 0\n\tbuffer_load_dwordx4 %1, %2, %3 offen lds" ::"s"(ldsaddr), "v"(voff), "s"(srd), "s"(soff));
    else asm volatile("buffer_load_dwordx4 %0, %1, %2 offen offset:%3 lds" ::"v"(voff), "s"(srd), "s"(soff), "n"(I * 1024));
  }
}
#define X_NT 0
#define WQ_NT 0
#define SL_NT 0
#define WO_NT 0
#define KVPOL "nt"
template <int I> DI void dma_piece_kv(unsigned ldsaddr, unsigned voff, srd_t srd, unsigned soff) {
  if constexpr (I == 0) asm volatile("s_mov_b32 m0, %0\n\ts_nop 0\n\tbuffer_load_dwordx4 %1, %2, %3 offen " KVPOL " lds" ::"s"(ldsaddr), "v"(voff), "s"(srd), "s"(soff));
  else asm volatile("buffer_load_dwordx4 %0, %1, %2 offen offset:%3 " KVPOL " lds" ::"v"(voff), "s"(srd), "s"(soff), "n"(I * 1024));
}
template <int I> DI void dma_m0_kv(unsigned ldsaddr, unsigned voff, srd_t srd, unsigned soff) {
  asm volatile("s_mov_b32 m0, %0\n\ts_nop 0\n\tbuffer_load_dwordx4 %1, %2, %3 offen offset:%4 " KVPOL " lds" ::"s"(ldsaddr), "v"(voff), "s"(srd), "s"(soff), "n"(I * 1024));
}
template <int N> DI void wait_bar() { asm volatile("s_waitcnt vmcnt(%0) lgkmcnt(0)\n\ts_barrier" ::"n"(N) : "memory"); }
template <int N> DI void wait_vm() { asm volatile("s_waitcnt vmcnt(%0)" ::"n"(N) : "memory"); }
DI lds_cptr lds_base(unsigned a) { asm volatile("" : "+v"(a)); return (lds_cptr)a; }
DI unsigned rfl(unsigned x) { return __builtin_amdgcn_readfirstlane(x); }
DI unsigned sel3(int c, unsigned a0, unsigned a1, unsigned a2) { return c == 0 ? a0 : (c == 1 ? a1 : a2); }
DI void ld_word(unsigned& d, const void* base, unsigned voff) { asm volatile("global_load_dword %0, %1, %2 sc1" : "=v"(d) : "v"(voff), "s"(base) : "memory"); }
DI void st_word(const void* base, unsigned voff, unsigned v) { asm volatile("global_store_dword %0, %1, %2 sc1" ::"v"(voff), "v"(v), "s"(base) : "memory"); }
template <int I> DI void ld_frag(bf16x8& d, srd_t srd, unsigned voff, unsigned soff) {
#define OPOL "sc1"
  asm volatile("buffer_load_dwordx4 %0, %1, %2, %3 offen offset:%4 " OPOL : "=v"(d) : "v"(voff), "s"(srd), "s"(soff), "n"(I * 1024) : "memory");
}
constexpr unsigned ACK_KEY = 0x9E3779B9u;
DI void poll_flag(const void* base, unsigned voff, unsigned f0, unsigned lo) {
#pragma unroll 1
  for (unsigned spins = 0; spins < (1u << 16); ++spins) {
    unsigned v;
    ld_word(v, base, voff);
    asm volatile("s_waitcnt vmcnt(0)" : "+v"(v)::"memory");
    const unsigned d = rfl(v) - f0;
    if (d >= lo && d <= 2u) break;
    __builtin_amdgcn_s_sleep(2);
  }
}
DI void poll_equal(const void* base, unsigned voff, unsigned want) {
#pragma unroll 1
  for (unsigned spins = 0; spins < (1u << 16); ++spins) {
    unsigned v;
    ld_word(v, base, voff);
    asm volatile("s_waitcnt vmcnt(0)" : "+v"(v)::"memory");
    if (rfl(v) == want) break;
    __builtin_amdgcn_s_sleep(2);
  }
}

struct Params {
  const void* x[2]; const void* k[2]; const void* v[2]; const void* wq[2]; const void* wo[2];
  void* ows; float* part; void* out; unsigned* sync;
};

constexpr unsigned KV_BYTES = 17 * SK * DH * 2;
constexpr unsigned QX = 135168;
constexpr unsigned GA = 73728, SLAB0 = GA + 20480, H0 = GA, H1W3 = 155648, DUMMY = 159744, CNT = 49152;
constexpr unsigned xslot(int c) { return (c % 3 == 0 ? 60u : c % 3 == 1 ? 76u : 132u) * 1024u; }
constexpr unsigned wslot(int j) { return (j % 5 == 0 ? 92u : j % 5 == 1 ? 104u : j % 5 == 2 ? 116u : j % 5 == 3 ? 148u : 48u) * 1024u; }
#define KVB0 1
#define KVB1 7

#define PSCHED 3
constexpr int wmax(int k) {
  if (k < -1) return -1;
  const int full = k + 4 < 19 ? k + 4 : 19;
  if (PSCHED == 1) return k == -1 ? 1 : k == 0 ? 3 : full;
  if (PSCHED == 2) return k == -1 ? 0 : k == 0 ? 2 : k == 1 ? 4 : full;
  if (PSCHED == 3) return k == -1 ? 1 : k == 0 ? 2 : k == 1 ? 4 : full;
  if (PSCHED == 4) return k == -1 ? 1 : k == 0 ? 4 : full;
  return full;
}
constexpr int xmax(int k) {
  if (k < -1) return -1;
  const int f0 = (k + 4) / 2 - ((k & 1) && k > 0 ? 0 : 0), full = k == -1 ? 1 : (f0 < 9 ? f0 : 9);
  if (PSCHED == 1) return k == -1 ? 0 : k == 0 ? 1 : full;
  if (PSCHED == 2) return k == -1 ? 0 : k == 0 ? 1 : k == 1 ? 1 : k == 2 ? 2 : k == 3 ? 3 : full;
  if (PSCHED == 3) return k == -1 ? 0 : k == 0 ? 1 : k == 1 ? 1 : full;
  if (PSCHED == 4) return k == -1 ? 0 : k == 0 ? 1 : full;
  return full;
}
constexpr int pro_allowed(int i) {
  int n = 0, need = 0;
  for (int k = -1; k < i; ++k) {
    for (int t = 0; t < 20; ++t) {
      if ((t & 1) == 0 && t / 2 > xmax(k - 1) && t / 2 <= xmax(k)) { n += 4; if (t / 2 == i / 2) need = n > need ? n : need; }
      if (t > wmax(k - 1) && t <= wmax(k)) { n += 3; if (t == i) need = n > need ? n : need; }
    }
    if (k == 17 || k == 18) n += 4;
  }
  return n - need;
}
constexpr unsigned OOB = 0x7ff00000u;
constexpr int req_pos(int e) {
  constexpr int t[15] = {19, 19, 19, 19, 19, 19, 19, 19, 19, 0, 1, 2, 4, 5, 6};
  return t[e];
}
constexpr int req_ord(int k) { return k < 5 ? k + 4 : (k < 9 ? k - 5 : k); }
constexpr unsigned wo_slot(int c) { return c == 16 || c == 23 ? 0u : c == 17 ? 5120u : c == 18 ? 49664u : GA + ((c < 16 ? c : c - 19) % 4) * 20480u; }
constexpr unsigned wo_gs(int c) { return c == 16 || c == 17 || c == 23 ? 12288u : 5120u; }
#define TD 3
#define TPOS 0
constexpr int obuf(int c) { return c < 15 ? c % 3 : (c - 15) % (TD + 1); }
constexpr int tail_pos(int e) {
  constexpr int t0[9] = {0, 1, 2, 3, 5, 7, 9, 11, 13};
  constexpr int t1[9] = {15, 16, 17, 18, 9, 10, 11, 12, 13};
  constexpr int t2[9] = {0, 2, 4, 6, 8, 10, 12, 14, 16};
  constexpr int t3[9] = {5, 6, 7, 8, 0, 1, 2, 3, 4};
  return TPOS == 0 ? t0[e] : TPOS == 1 ? t1[e] : TPOS == 2 ? t2[e] : t3[e];
}
constexpr int tail_cnt(int c) {
  int n = 0, need = 0;
  auto O = [&](int k) { n += 4; if (k <= c + 1) need = n; };
  auto W = [&](int k) { n += 5; if (k <= c + 2) need = n; };
  for (int k = 15; k <= 15 + TD; ++k) O(k);
  for (int k = 15; k <= c; ++k) {
    const bool o_first = tail_pos(0) < tail_pos(4);
    if (o_first && k > 15 && k + TD < 24) O(k + TD);
    if (k + 4 < 24) W(k + 4);
    if (!o_first && k > 15 && k + TD < 24) O(k + TD);
  }
  return n - need;
}
#define DRAIN_MASK "0x20001"
constexpr int O_AUX = 16;

__global__ __launch_bounds__(512, 2) void k1_attn(Params p) {
  extern __shared__ __attribute__((aligned(16))) char smem[];
  const int tid = threadIdx.x, lane = tid & 63, r = lane & 31, hh = lane >> 5;
  const unsigned wv = rfl(tid >> 6), g = wv >> 2, w = wv & 3;
  const int bid = blockIdx.x, xg = bid & 7, idx = bid >> 3;
  const int br = xg >> 2, b = idx >> 2, h1 = (xg & 3) * 4 + (idx & 3), h2 = h1 + 16;
  const unsigned lds0 = (unsigned)(uintptr_t)smem;
  lds_cptr L = (lds_cptr)smem;
  lds_ptr LW = (lds_ptr)smem;
  const unsigned gi = idx & 3, hq = xg & 3;
  const char* sync_all = (const char*)p.sync;
  const unsigned sy_self = bid * 512, sy_grp = (b * 32 + xg) * 512, sy_red = (idx * 8 + (xg & 4)) * 512;
  const srd_t sk = mksrd((const char*)p.k[br] + (size_t)(b * NH + h1) * SK * DH * 2, KV_BYTES);
  const srd_t sv = mksrd((const char*)p.v[br] + (size_t)(b * NH + h1) * SK * DH * 2, KV_BYTES);
  unsigned k_off[3], v_off[3];
#pragma unroll
  for (int i = 0; i < 3; ++i) {
    const int pp = 64 * i + lane, row = pp / 24, pos = pp % 24;
    v_off[i] = row * 384 + (pos ^ (((row >> 1) & 1) << 2)) * 16 - 1024 * i;
    const int f = (4 * w + (row >> 1)) & 7;
    k_off[i] = row * 384 + ((pos & ~7) | ((pos & 7) ^ f)) * 16 - 1024 * i;
  }
  auto kv_so = [&](int T) AI { return (unsigned)((T >> 4) * (16 * SK * DH * 2) + (T & 15) * 12288) + w * 3072; };
  const int blk = (lane >> 4) & 1, q4 = (lane & 15) >> 2, p4 = lane & 3, bit = (q4 >> 1) & 1;
  const int trlo = blk * 32 + (p4 & 1) * 16 + (p4 >> 1) * 8;
  const unsigned x_sw = (hh ^ ((r >> 1) & 7)) * 16;
  const unsigned hq0 = H0 + w * 4096, hq1 = w < 3 ? SLAB0 + w * 20480 + 16384 : H1W3, hq2 = SLAB0 + w * 4096;

  if (g == 0) {
#define PRIO_A0 0
#define PRIO_A1 2
#define PRIO_G0 2
#define PRIO_G1 0
    __builtin_amdgcn_s_setprio(PRIO_A0);
    unsigned f0v;
    ld_word(f0v, sync_all, sy_self);
    bf16x8 qf[12];
    {
      const unsigned qh = w >> 1, dh = w & 1;
      unsigned wq_rd[3];
#pragma unroll
      for (int t = 0; t < 3; ++t) { const unsigned dt = 3 * dh + t; wq_rd[t] = (8 * hh + q4) * 384 + ((dt & 1) ^ bit) * 64 + trlo + (dt >> 1) * 128; }
      const unsigned xa_rd = (64 * qh + r) * 128 + x_sw;
      f32x16 qa[2][3];
      bf16x8 xf[3][2], wf[3][3];
      sfor<0, 20>([&](auto I) AI {
        constexpr int i = decltype(I)::value, b0 = (2 * i) % 3, b1 = (2 * i + 1) % 3, bp = (2 * i + 2) % 3;

        if constexpr (i == 19) { wait_vm<9>(); asm volatile("" : "+v"(f0v)); }
        wait_bar<63>();
        const lds_cptr wa[3] = {lds_base(lds0 + wq_rd[0] + wslot(i)), lds_base(lds0 + wq_rd[1] + wslot(i)), lds_base(lds0 + wq_rd[2] + wslot(i))};
        const lds_cptr xa0 = lds_base(lds0 + (xa_rd ^ ((i & 1) * 64)) + xslot(i / 2)), xa1 = lds_base(lds0 + (xa_rd ^ ((i & 1) * 64 + 32)) + xslot(i / 2));
        auto ldw = [&](int ks, int t) AI { lds_cptr a = wa[t] + ks * 16 * 384; return cat(lds_tr(a), lds_tr(a + 4 * 384)); };
        sfor<0, 12>([&](auto GG) AI {
          constexpr int gp = decltype(GG)::value, t = (gp % 6) >> 1, c = gp & 1;
          if constexpr (gp < 6) { if constexpr (i > 0) qa[c][t] = MFMA(wf[bp][t], xf[bp][c], qa[c][t]); }
          else qa[c][t] = MFMA(wf[b0][t], xf[b0][c], i == 0 ? f32x16{} : qa[c][t]);
          if constexpr (gp == 0) xf[b0][0] = lds_rd128(xa0);
          if constexpr (gp == 1) wf[b0][0] = ldw(0, 0);
          if constexpr (gp == 2) xf[b0][1] = lds_rd128(xa0 + 4096);
          if constexpr (gp == 3) wf[b0][1] = ldw(0, 1);
          if constexpr (gp == 4) wf[b0][2] = ldw(0, 2);
          if constexpr (gp == 5) { xf[b1][0] = lds_rd128(xa1); xf[b1][1] = lds_rd128(xa1 + 4096); }
          if constexpr (gp == 6) wf[b1][0] = ldw(1, 0);
          if constexpr (gp == 7) wf[b1][1] = ldw(1, 1);
          if constexpr (gp == 8) wf[b1][2] = ldw(1, 2);
          if constexpr (gp >= 6) {
            constexpr int pc = (gp - 6) % 3, half = (gp - 6) / 3;
            if constexpr (i == KVB0 && half == 0) dma_m0_kv<pc>(lds0 + w * 3072, k_off[pc], sk, kv_so(0));
            if constexpr (i == KVB0 && half == 1) dma_m0_kv<pc>(lds0 + 12288 + w * 3072, v_off[pc], sv, kv_so(0));
            if constexpr (i == KVB1 && half == 0) dma_m0_kv<pc>(lds0 + 24576 + w * 3072, k_off[pc], sk, kv_so(1));
            if constexpr (i == KVB1 && half == 1) dma_m0_kv<pc>(lds0 + 24576 + 12288 + w * 3072, v_off[pc], sv, kv_so(1));
          }
          SB();
        });
      });
#pragma unroll
      for (int gp = 0; gp < 6; ++gp) qa[gp & 1][gp >> 1] = MFMA(wf[39 % 3][gp >> 1], xf[39 % 3][gp & 1], qa[gp & 1][gp >> 1]);
      const unsigned xw = QX + (w ^ 1) * 6144 + lane * 16, xr_ = QX + w * 6144 + lane * 16;
      if (dh == 0) {
#pragma unroll
        for (int i = 0; i < 6; ++i) { *(lds_b8)(LW + xw + i * 1024) = pack8(qa[1][i >> 1], i & 1); qf[i] = pack8(qa[0][i >> 1], i & 1); }
        wait_bar<63>();
#pragma unroll
        for (int i = 0; i < 6; ++i) qf[6 + i] = lds_rd128(L + xr_ + i * 1024);
      } else {
#pragma unroll
        for (int i = 0; i < 6; ++i) { *(lds_b8)(LW + xw + i * 1024) = pack8(qa[0][i >> 1], i & 1); qf[6 + i] = pack8(qa[1][i >> 1], i & 1); }
        wait_bar<63>();
#pragma unroll
        for (int i = 0; i < 6; ++i) qf[i] = lds_rd128(L + xr_ + i * 1024);
      }
      sfor<0, 3>([&](auto P) AI { dma_m0_kv<decltype(P)::value>(lds0 + 49152 + w * 3072, k_off[decltype(P)::value], sk, kv_so(2)); });
    }
    const unsigned f0self = rfl(f0v);
    __builtin_amdgcn_s_setprio(PRIO_A1);
    int k_rd[4], v_rd[2];
#pragma unroll
    for (int s = 0; s < 4; ++s) k_rd[s] = r * 384 + (((2 * s + hh) ^ ((r >> 1) & 7)) * 16);
#pragma unroll
    for (int v = 0; v < 2; ++v) v_rd[v] = 12288 + (4 * hh + q4) * 384 + (v ^ bit) * 64 + trlo;
    unsigned s0 = 0, s1 = 24576, s2 = 49152;
    auto xhalf_max = [&](float mx) AI {
      auto rr = __builtin_amdgcn_permlane32_swap(__float_as_uint(mx), __float_as_uint(mx), false, false);
      return fmaxf(__uint_as_float(rr[0]), __uint_as_float(rr[1]));
    };
#pragma unroll 1
    for (int item = 0; item < 2; ++item) {
      f32x16 o[6], sa, sb;
      float m, l = 0.f, mx;
      {
        const lds_cptr Lk = L + s0;
        bf16x8 kf[12];
#pragma unroll
        for (int ks = 0; ks < 12; ++ks) kf[ks] = lds_rd128(Lk + k_rd[ks & 3] + (ks >> 2) * 128);
        sfor<0, 12>([&](auto KS) AI { constexpr int ks = decltype(KS)::value;
          sa = MFMA(kf[ks], qf[ks], ks == 0 ? f32x16{} : sa);
          if constexpr (ks < 6) { o[ks] = f32x16{}; asm volatile("" : "+v"(o[ks])); }
          SB();
        });
        mx = fmaxf(fmaxf(sa[0], sa[1]), sa[2]);
#pragma unroll
        for (int i = 3; i < 15; i += 2) mx = fmaxf(fmaxf(mx, sa[i]), sa[i + 1]);
        mx = xhalf_max(fmaxf(mx, sa[15]));
        m = mx;
      }
      bool need = false;
      bf16x8 vfl[3] = {bf16x8{}, bf16x8{}, bf16x8{}}, pfl = bf16x8{};
      if (item == 0) wait_bar<6>(); else wait_bar<63>();
      auto step = [&](auto LAST, int T, f32x16& sc, f32x16& sn) AI {
        constexpr bool last = decltype(LAST)::value;

        const lds_cptr Lk = L + s1, Lv = L + s0;
        bf16x8 kf[12], vf[9];
        auto ldk = [&](int ks) AI { return lds_rd128(Lk + k_rd[ks & 3] + (ks >> 2) * 128); };
        auto ldv = [&](int j) AI {
          lds_cptr a = Lv + v_rd[(j % 6) & 1] + ((j % 6) >> 1) * 128 + (j / 6) * 16 * 384;
          return cat(lds_tr(a), lds_tr(a + 8 * 384));
        };
        if constexpr (!last) {
#pragma unroll
          for (int ks = 0; ks < 5; ++ks) kf[ks] = ldk(ks);
        }
        const float mo = m;
        if (need) m = fmaxf(m, mx);
        const float mc = m * QSCALE;
        unsigned pw[8];
        float ps0 = 0.f, ps1 = 0.f, mn = 0.f;
        auto fill = [&](auto GG) AI {
          constexpr int gp = decltype(GG)::value;
          if constexpr (gp == 0) sc[0] = __builtin_fmaf(sc[0], QSCALE, -mc);
          if constexpr (gp < 15) { sc[gp + 1] = __builtin_fmaf(sc[gp + 1], QSCALE, -mc); asm volatile("" : "+v"(sc[gp + 1])); }
          if constexpr (gp < 16) sc[gp] = __builtin_amdgcn_exp2f(sc[gp]);
          if constexpr (gp >= 2 && gp < 18 && (gp & 1) == 0) pw[gp / 2 - 1] = cvtpk(sc[gp - 2], sc[gp - 1]);
          if constexpr (gp >= 2 && gp < 18) { if constexpr (gp & 1) { ps1 += sc[gp - 2]; asm volatile("" : "+v"(ps1)); } else { ps0 += sc[gp - 2]; asm volatile("" : "+v"(ps0)); } }
          if constexpr (!last) {
            if constexpr (gp == 18) { mn = fmaxf(fmaxf(sn[0], sn[1]), sn[2]); mn = fmaxf(fmaxf(mn, sn[3]), sn[4]); asm volatile("" : "+v"(mn)); }
            if constexpr (gp >= 19 && gp < 22) { constexpr int i = 5 + 4 * (gp - 19); mn = fmaxf(fmaxf(fmaxf(fmaxf(mn, sn[i]), sn[i + 1]), sn[i + 2]), sn[i + 3 < 16 ? i + 3 : 15]); asm volatile("" : "+v"(mn)); }
            if constexpr (gp == 22) { mn = xhalf_max(mn); asm volatile("" : "+v"(mn)); }
          }
        };
        sfor<0, 3>([&](auto J) AI { constexpr int j = decltype(J)::value;
          o[3 + j] = MFMA(vfl[j], pfl, o[3 + j]);
          fill(ic<j>{});
          SB();
        });
        if (__builtin_expect(need, 0)) {
          const float f = __builtin_amdgcn_exp2f((mo - m) * QSCALE);
          l *= f;
#pragma unroll
          for (int d = 0; d < 6; ++d)
#pragma unroll
            for (int i = 0; i < 16; ++i) o[d][i] *= f;
        }
        SB();
        sfor<0, 12>([&](auto KS) AI {
          constexpr int ks = decltype(KS)::value;
          if constexpr (!last) {
            sn = MFMA(kf[ks], qf[ks], ks == 0 ? f32x16{} : sn);
            if constexpr (ks + 5 < 12) kf[ks + 5] = ldk(ks + 5);
          }
          if constexpr (ks >= 6) vf[ks - 6] = ldv(ks - 6);
          fill(ic<3 + ks>{});
          SB();
        });
        const bf16x8 pf0 = __builtin_bit_cast(bf16x8, u32x4{pw[0], pw[1], pw[2], pw[3]});
        sfor<0, 9>([&](auto J) AI { constexpr int j = decltype(J)::value;
          if constexpr (j == 6) pfl = __builtin_bit_cast(bf16x8, u32x4{pw[4], pw[5], pw[6], pw[7]});
          o[j % 6] = MFMA(vf[j], j < 6 ? pf0 : pfl, o[j % 6]);
          if constexpr (j + 6 < 9) vf[j + 6] = ldv(j + 6);
          else if constexpr (j + 6 < 12) vfl[j - 3] = ldv(j + 6);
          fill(ic<15 + j>{});
          SB();
        });
        l += ps0 + ps1;
        if constexpr (!last) { mx = mn; need = __any(mx - m > THR); }
        asm volatile("s_bitcmp1_b32 " DRAIN_MASK ", %0\n\ts_cbranch_scc0 1f\n\ts_waitcnt vmcnt(0)\n1:\n\ts_waitcnt lgkmcnt(0)\n\ts_barrier" ::"s"(T) : "scc", "memory");
        const unsigned tmp = s0; s0 = s1; s1 = s2; s2 = tmp;
      };
      const int T0 = 16 * item;
#pragma unroll 1
      for (int T = 0; T < 14; T += 2) { step(std::false_type{}, T0 + T, sa, sb); step(std::false_type{}, T0 + T + 1, sb, sa); }
      step(std::false_type{}, T0 + 14, sa, sb);
      step(std::true_type{}, T0 + 15, sb, sa);
#pragma unroll
      for (int j = 0; j < 3; ++j) o[3 + j] = MFMA(vfl[j], pfl, o[3 + j]);
      if (item == 0) {
#pragma unroll
        for (int i = 0; i < 12; ++i) qf[i] = lds_rd128(L + sel3(i >> 2, hq0, hq1, hq2) + (i & 3) * 1024 + lane * 16);
      }
      {
        auto rr = __builtin_amdgcn_permlane32_swap(__float_as_uint(l), __float_as_uint(l), false, false);
        const float inv = 1.0f / (__uint_as_float(rr[0]) + __uint_as_float(rr[1]));
        const srd_t so = mksrd((const char*)p.ows + ((size_t)((br * NB + b) * NH + h1 + 16 * item) * 4 + w) * 12288, 12288);
        const unsigned hb0 = item == 0 ? hq0 : w * 12288, hb1 = item == 0 ? hq1 : w * 12288 + 4096, hb2 = item == 0 ? hq2 : w * 12288 + 8192;
#pragma unroll
        for (int i = 0; i < 12; ++i) {
          const bf16x8 of = pack8(o[i >> 1], i & 1, inv);
          __builtin_amdgcn_raw_buffer_store_b128(__builtin_bit_cast(u32x4, of), so, lane * 16 + i * 1024, 0, O_AUX);
          *(lds_b8)(LW + sel3(i >> 2, hb0, hb1, hb2) + (i & 3) * 1024 + lane * 16) = of;
        }
      }
    }
    typedef __attribute__((address_space(3))) unsigned* lds_u32;
    if (w == 0 && lane == 0) *(lds_u32)(LW + CNT) = 0;
    wait_bar<63>();
    asm volatile("s_waitcnt vmcnt(0)" ::: "memory");
    if (lane == 0) {
      const unsigned n = __hip_atomic_fetch_add((lds_u32)(LW + CNT), 1u, __ATOMIC_RELAXED, __HIP_MEMORY_SCOPE_WORKGROUP);
      if (n == 3) st_word(sync_all, sy_self, f0self + 2);
    }
  } else {
    const srd_t sx = mksrd((const char*)p.x[br] + (size_t)b * SQ * DM * 2, SQ * DM * 2);
    const srd_t swq = mksrd((const char*)p.wq[br] + h1 * DH * 2, DM * NH * DH * 2 - h1 * DH * 2);
    __builtin_amdgcn_s_setprio(PRIO_G0);
    unsigned f0v;
    ld_word(f0v, sync_all, (lane & 3) == 0 ? sy_grp + w * 4096 : ((lane & 3) == 1 ? sy_red + w * 512 + 256 : ((lane & 3) == 2 ? sy_self : sy_self + 256)));
    bf16x8 xr[40];
    const unsigned sl_off = (lane >> 3) * (NH * DH * 2) + (((lane & 7) ^ (((lane >> 4) & 1) << 2)) * 16);
    auto slab_piece = [&](unsigned dst, unsigned so, auto I) AI { constexpr int i = decltype(I)::value;
      dma_piece<i, SL_NT>(dst, sl_off, swq, so + i * (8 * NH * DH * 2 - 1024)); };
#define ROTM 5
    const unsigned rot = ROTM == 0 ? 0 : ROTM == 1 ? 2 * (((5 * b) / 4 + (5 * gi) / 2) % 10) : ROTM == 2 ? 10 * ((b + gi) & 1) : ROTM == 3 ? 2 * ((5 * ((b + gi) & 3)) / 2)
                       : ROTM == 4 ? 2 * ((b + gi) & 1) : ROTM == 5 ? 2 * ((b + gi) & 3) : ROTM == 6 ? 2 * ((b + gi) % 5) : ROTM == 7 ? 2 * ((b + 2 * gi) & 7) : ROTM == 8 ? 2 * ((b + gi) % 6) : ROTM == 9 ? 2 * ((b + gi) % 3) : 2 * ((b + 3 * gi) % 10);
    auto stg = [&](unsigned i) AI { const unsigned t = rot + i; return t >= 40 ? t - 40 : (t >= 20 ? t - 20 : t); };
    auto slab_so = [&](int pp, int j) AI { return (unsigned)(32 * stg(4 * j + w) * (NH * DH * 2) + (16 * DH + 64 * pp) * 2); };
    {
      unsigned wq_off[3];
#pragma unroll
      for (int i = 0; i < 3; ++i) { const int pp = 64 * i + lane, row = pp / 24, pos = pp % 24; wq_off[i] = row * (NH * DH * 2) + (pos ^ (((row >> 1) & 1) << 2)) * 16 - 1024 * i; }
      const unsigned x_off = (lane >> 3) * (DM * 2) + (((lane & 7) ^ (lane >> 4)) * 16);
      auto issue_x = [&](auto C) AI { constexpr int c = decltype(C)::value;
        const unsigned so = 32 * w * (DM * 2) + stg(2 * c) * 64;
        sfor<0, 4>([&](auto P) AI { constexpr int pc = decltype(P)::value;
          dma_piece<pc, (c < X_NT)>(lds0 + xslot(c) + w * 4096, (pc & 1) ? (x_off ^ 64) : x_off, sx, so + pc * (8 * DM * 2 - 1024)); });
      };
      auto issue_w = [&](auto J) AI { constexpr int j = decltype(J)::value;
        sfor<0, 3>([&](auto P) AI { constexpr int pc = decltype(P)::value;
          dma_piece<pc, (j < WQ_NT)>(lds0 + wslot(j) + w * 3072, wq_off[pc], swq, (unsigned)((stg(j) * 32 + 8 * w) * (NH * DH * 2))); });
      };
      auto issue_slot = [&](auto K) AI { constexpr int k = decltype(K)::value;
        sfor<0, 20>([&](auto T) AI { constexpr int t = decltype(T)::value;
          if constexpr ((t & 1) == 0 && t / 2 > xmax(k - 1) && t / 2 <= xmax(k)) issue_x(ic<t / 2>{});
          if constexpr (t > wmax(k - 1) && t <= wmax(k)) issue_w(ic<t>{});
        });
      };
      issue_slot(ic<-1>{});
      sfor<0, 20>([&](auto I) AI {
        constexpr int i = decltype(I)::value;

        wait_bar<pro_allowed(i)>();
        if constexpr (i == 0) {
          asm volatile("" : "+v"(f0v));
          if (lane < 2) st_word(sync_all, sy_self + 128 + lane * 256 + w * 4, f0v ^ ACK_KEY);
        }
        issue_slot(I);
        if constexpr (i == 17 || i == 18) sfor<0, 4>([&](auto P) AI { slab_piece(lds0 + SLAB0 + (i - 17) * 20480 + w * 4096, slab_so(0, i - 17), P); });
        xr[2 * i] = lds_rd128(lds_base(lds0 + 32 * w * 128 + r * 128 + (x_sw ^ ((i & 1) * 64)) + xslot(i / 2)));
        xr[2 * i + 1] = lds_rd128(lds_base(lds0 + 32 * w * 128 + r * 128 + (x_sw ^ ((i & 1) * 64 + 32)) + xslot(i / 2)));
      });
    }
    const unsigned f0part = __builtin_amdgcn_readlane(f0v, 0), f0red = __builtin_amdgcn_readlane(f0v, 1), f0self = __builtin_amdgcn_readlane(f0v, 2), f0sred = __builtin_amdgcn_readlane(f0v, 3);
    wait_bar<63>();
    wait_bar<4>();
    __builtin_amdgcn_s_setprio(PRIO_G1);
    unsigned wrd[2];
#pragma unroll
    for (int v = 0; v < 2; ++v) wrd[v] = SLAB0 + (8 * hh + q4) * 128 + (v ^ bit) * 64 + trlo;
    unsigned c0 = 0, c1 = 1, c2 = 2;
    auto kv_piece = [&](int T, auto N) AI { constexpr int n = decltype(N)::value;
      if constexpr (n < 3) dma_piece_kv<n>(T + 3 < 32 ? lds0 + c0 * 24576 + w * 3072 : lds0 + DUMMY, k_off[n], sk, kv_so(T + 3));
      else dma_piece_kv<n - 3>(T + 2 < 32 ? lds0 + c2 * 24576 + 12288 + w * 3072 : lds0 + DUMMY, v_off[n - 3], sv, kv_so(T + 2));
    };
#pragma unroll 1
    for (int ps = 0; ps < 3; ++ps) {
      f32x16 acc[2];
      sfor<0, 5>([&](auto J) AI {
        constexpr int j = decltype(J)::value;
        const int T = 5 * ps + j;

        const lds_cptr a0 = lds_base(lds0 + wrd[0] + c0 * 20480), a1 = lds_base(lds0 + wrd[1] + c0 * 20480);
        auto ldw = [&](int ks, int v) AI { lds_cptr a = (v ? a1 : a0) + ks * 2048; return cat(lds_tr(a), lds_tr(a + 512)); };
        const bool more = T + 2 < 15;
        const int pn = j < 3 ? ps : ps + 1, jn = (j + 2) % 5;
        const unsigned sdst = more ? lds0 + SLAB0 + c2 * 20480 + w * 4096 : lds0 + DUMMY, sso = more ? slab_so(pn, jn) : OOB;
        bf16x8 wf[3][2];
        wf[0][0] = ldw(0, 0); wf[0][1] = ldw(0, 1); wf[1][0] = ldw(1, 0); wf[1][1] = ldw(1, 1);
        SB();
        sfor<0, 16>([&](auto GG) AI {
          constexpr int gp = decltype(GG)::value, ks = gp >> 1, v = gp & 1;
          acc[v] = MFMA(wf[ks % 3][v], xr[8 * j + ks], (j == 0 && ks == 0) ? f32x16{} : acc[v]);
          if constexpr (ks + 2 < 8) wf[(ks + 2) % 3][v] = ldw(ks + 2, v);
          if constexpr (gp >= 0 && gp < 3) kv_piece(T, ic<gp>{});
          if constexpr (gp >= 4 && gp < 7) kv_piece(T, ic<gp - 1>{});
          if constexpr (gp >= 8 && gp < 12) slab_piece(sdst, sso, ic<gp - 8>{});
          SB();
        });
        if constexpr (j == 4) {
          const unsigned hd = sel3(ps, hq0, hq1, hq2);
#pragma unroll
          for (int v = 0; v < 2; ++v)
#pragma unroll
            for (int s = 0; s < 2; ++s) *(lds_b8)(LW + hd + (2 * v + s) * 1024 + lane * 16) = pack8(acc[v], s);
        }
        wait_bar<10>();
        const unsigned tmp = c0; c0 = c1; c1 = c2; c2 = tmp;
      });
    }
    auto kv_all = [&](int T) AI {
      sfor<0, 6>([&](auto N) AI { kv_piece(T, N); });
      const unsigned tmp = c0; c0 = c1; c1 = c2; c2 = tmp;
    };
    const srd_t swo = mksrd((const char*)p.wo[br] + gi * 320, NH * DH * DM * 2 - gi * 320);
    const srd_t sof = mksrd((const char*)p.ows + (size_t)(br * NB + b) * NH * 49152, NH * 49152);
    unsigned wo_off[5];
#pragma unroll
    for (int i = 0; i < 5; ++i) { const int pp = 64 * i + lane; wo_off[i] = (pp / 20) * (DM * 2) + (pp % 20) * 16 - (i < 4 ? 1024 * i : 0); }
    const unsigned rb = b % 3, rq = b / 3;
    auto pos = [&](unsigned q) AI { const unsigned t = q + rb; return t >= 3 ? t - 3 : t; };
    auto head_of = [&](auto C) AI {
      constexpr int c = decltype(C)::value, u = (c % 12) / 3;
      if constexpr (u == 0) return (unsigned)(h1 + 16 * (c / 12));
      else { const unsigned t0 = u - 1 + rq, t = t0 >= 3 ? t0 - 3 : t0; return (unsigned)((xg & 3) * 4 + ((gi + 1 + t) & 3) + 16 * (c / 12)); }
    };
    auto wo_piece = [&](auto C, auto N) AI {
      constexpr int c = decltype(C)::value, n = decltype(N)::value;
      const unsigned so = (head_of(C) * DH + pos(c % 3) * 64 + 16 * w) * (DM * 2), dst = lds0 + wo_slot(c) + w * wo_gs(c);
      if constexpr (n < 4) dma_piece<n, WO_NT>(dst, wo_off[n], swo, so); else dma_piece<0, WO_NT>(dst + 4096, wo_off[4], swo, so);
    };
    auto wo_all = [&](auto C) AI { sfor<0, 5>([&](auto N) AI { wo_piece(C, N); }); };
    bf16x8 of[TD + 1][4];
    auto o_frag = [&](auto C, auto S) AI {
      constexpr int c = decltype(C)::value, s2 = decltype(S)::value;
      ld_frag<s2>(of[obuf(c)][s2], sof, lane * 16, head_of(C) * 49152 + w * 12288 + pos(c % 3) * 4096);
    };
    auto o_all = [&](auto C) AI { sfor<0, 4>([&](auto S) AI { o_frag(C, S); }); };
    auto own_read = [&](unsigned b0, unsigned b1, unsigned b2) AI {
#pragma unroll
      for (int q = 0; q < 3; ++q) {
        const lds_cptr a = lds_base(lds0 + sel3(pos(q), b0, b1, b2) + lane * 16);
#pragma unroll
        for (int s2 = 0; s2 < 4; ++s2) of[q][s2] = lds_rd128(a + s2 * 1024);
      }
    };
    f32x16 acc[5];
    bf16x8 wf[2][5];
    const unsigned wo_rd = (8 * hh + q4) * 320 + trlo;
    auto wf_first = [&](auto C) AI {
      const lds_cptr a0 = lds_base(lds0 + wo_rd + wo_slot(decltype(C)::value));
#pragma unroll
      for (int t = 0; t < 5; ++t) wf[0][t] = cat(lds_tr(a0 + t * 64), lds_tr(a0 + t * 64 + 4 * 320));
    };
    unsigned flagv;
    auto chunk = [&](auto C) AI {
      constexpr int c = decltype(C)::value, P = obuf(c);
      constexpr bool kv = c < 12, quiet = c >= 12 && c < 15;
      constexpr int wn = c < 12 ? c + 3 : c + 4;
      constexpr int on = c < 12 ? c + 2 : c + TD;
      constexpr bool wo_on = !quiet && wn < 24, o_on = !quiet && on < 24 && (on % 12) / 3 != 0 && c != 15;
      constexpr bool pre = c + 1 < 24;
      const int T = 20 + c;
      if constexpr (c == 0) ld_word(flagv, sync_all, sy_grp + w * 4096);
      const lds_cptr Lb = lds_base(lds0 + wo_rd + wo_slot(c)), Ln = lds_base(lds0 + wo_rd + wo_slot(c + 1));
      SB();
      sfor<0, 20>([&](auto GG) AI {
        constexpr int gp = decltype(GG)::value, s2 = gp / 5, t = gp % 5;
        acc[t] = MFMA(wf[s2 & 1][t], of[P][s2], acc[t]);
        if constexpr (s2 + 1 < 4) { lds_cptr a = Lb + t * 64 + (s2 + 1) * wo_gs(c); wf[(s2 + 1) & 1][t] = cat(lds_tr(a), lds_tr(a + 4 * 320)); }
        else if constexpr (pre) { lds_cptr a = Ln + t * 64; wf[0][t] = cat(lds_tr(a), lds_tr(a + 4 * 320)); }
        sfor<0, 15>([&](auto E) AI { constexpr int k = decltype(E)::value, e = req_ord(k);
          if constexpr ((c < 12 ? req_pos(e) : (e < 9 ? tail_pos(e < 9 ? e : 0) : -1)) == gp) {
            if constexpr (e < 4) { if constexpr (o_on) o_frag(ic<on>{}, ic<e>{}); }
            else if constexpr (e < 9) { if constexpr (wo_on) wo_piece(ic<wn>{}, ic<e - 4>{}); }
            else { if constexpr (kv) kv_piece(T, ic<e - 9>{}); }
          }
        });
        SB();
      });
      if constexpr (kv) { const unsigned tmp = c0; c0 = c1; c1 = c2; c2 = tmp; }
      constexpr int cnt = c >= 15 ? tail_cnt(c) : (kv ? 6 : 0) + (wo_on ? 5 : 0) + (o_on ? 4 : 0), nx = obuf(c + 1);
      if constexpr (c == 0) {
        asm volatile("s_waitcnt vmcnt(%1) lgkmcnt(0)" : "+v"(flagv) : "n"(cnt) : "memory");
        const unsigned d = rfl(flagv) - f0part;
        if (d - 1u >= 2u) poll_flag(sync_all, sy_grp + w * 4096, f0part, 1u);
        asm volatile("s_barrier" ::: "memory");
      } else if constexpr (!quiet && c < 23) {
        asm volatile("s_waitcnt vmcnt(%4) lgkmcnt(0)\n\ts_barrier" : "+v"(of[nx][0]), "+v"(of[nx][1]), "+v"(of[nx][2]), "+v"(of[nx][3]) : "n"(cnt) : "memory");
      }
    };
    kv_all(15);
    wait_bar<6>();
    wait_bar<6>();
    kv_all(16);
    own_read(hq0, hq1, hq2);
#pragma unroll
    for (int t = 0; t < 5; ++t) acc[t] = f32x16{};
    wait_bar<6>();
    {
      unsigned ackv;
      ld_word(ackv, sync_all, (lane & 1) == 0 ? sy_grp + w * 4096 + 128 + gi * 4 : sy_red + w * 512 + 384 + hq * 4);
      kv_all(17);
      wo_all(ic<0>{});
      asm volatile("s_waitcnt vmcnt(11) lgkmcnt(0)" : "+v"(ackv)::"memory");
      if (__builtin_amdgcn_readlane(ackv, 0) != (f0self ^ ACK_KEY)) poll_equal(sync_all, sy_grp + w * 4096 + 128 + gi * 4, f0self ^ ACK_KEY);
      if (__builtin_amdgcn_readlane(ackv, 1) != (f0sred ^ ACK_KEY)) poll_equal(sync_all, sy_red + w * 512 + 384 + hq * 4, f0sred ^ ACK_KEY);
      asm volatile("s_barrier" ::: "memory");
    }
    kv_all(18);
    wo_all(ic<1>{});
    if (w == 0) { if (lane == 0) st_word(sync_all, sy_self, f0self + 1); wait_bar<12>(); } else wait_bar<11>();
    kv_all(19);
    wo_all(ic<2>{});
    wf_first(ic<0>{});
    wait_bar<11>();
    sfor<0, 12>([&](auto C) AI { chunk(C); });
    wo_all(ic<15>{});
    wait_bar<0>();
    own_read(w * 12288, w * 12288 + 4096, w * 12288 + 8192);
    asm volatile("s_waitcnt lgkmcnt(0)" : "+v"(of[0][0]), "+v"(of[0][1]), "+v"(of[0][2]), "+v"(of[0][3]), "+v"(of[1][0]), "+v"(of[1][1]), "+v"(of[1][2]), "+v"(of[1][3]), "+v"(of[2][0]), "+v"(of[2][1]), "+v"(of[2][2]), "+v"(of[2][3])::"memory");
    wo_all(ic<16>{}); wo_all(ic<17>{}); wo_all(ic<18>{});
    sfor<12, 15>([&](auto C) AI { chunk(C); });
    poll_flag(sync_all, sy_grp + w * 4096, f0part, 2u);
    asm volatile("s_barrier" ::: "memory");
    sfor<15, 16 + TD>([&](auto C) AI { o_all(C); });
    asm volatile("s_waitcnt vmcnt(%4)" : "+v"(of[0][0]), "+v"(of[0][1]), "+v"(of[0][2]), "+v"(of[0][3]) : "n"(4 * TD) : "memory");
    sfor<15, 24>([&](auto C) AI { chunk(C); });
    {
      const unsigned grp = r >> 3, l16 = (r & 7) + 8 * hh;
      const srd_t sbox = mksrd(p.part, 256 * 40960);
      const unsigned own = GA + w * 5120 + l16 * 16;
      if (grp != hq) {
        const unsigned st_off = ((unsigned)(idx * 8 + (xg & 4)) + grp) * 40960 + (hq * 4 + w) * 2560 + l16 * 16;
#pragma unroll
        for (int j = 0; j < 10; ++j) __builtin_amdgcn_raw_buffer_store_b128(__builtin_bit_cast(u32x4, pack8(acc[j >> 1], j & 1)), sbox, st_off + j * 256, 0, 16);
      } else {
#pragma unroll
        for (int t = 0; t < 5; ++t)
#pragma unroll
          for (int u = 0; u < 4; ++u)
            *(__attribute__((address_space(3))) f32x4*)(LW + own + (4 * t + u) * 256) = f32x4{acc[t][4 * u], acc[t][4 * u + 1], acc[t][4 * u + 2], acc[t][4 * u + 3]};
      }
      wait_bar<0>();
      if (w == 0 && lane == 0) st_word(sync_all, sy_self + 256, f0sred + 1);
      f32x4 mine[3][2];
#pragma unroll
      for (int i = 0; i < 3; ++i)
#pragma unroll
        for (int h = 0; h < 2; ++h) mine[i][h] = *(const __attribute__((address_space(3))) f32x4*)(L + own + grp * 512 + i * 2048 + h * 256);
      poll_flag(sync_all, sy_red + w * 512 + 256, f0red, 1u);
      asm volatile("s_barrier" ::: "memory");
      bf16x8 in[3][3];
      const unsigned ld_off = w * 2560 + grp * 256 + l16 * 16, ld_off2 = grp < 2 ? ld_off : OOB;
      sfor<0, 3>([&](auto S) AI { constexpr int sr = decltype(S)::value;
        const unsigned so = bid * 40960 + ((hq + 1 + sr) & 3) * 10240;
        ld_frag<0>(in[sr][0], sbox, ld_off, so); ld_frag<1>(in[sr][1], sbox, ld_off, so); ld_frag<2>(in[sr][2], sbox, ld_off2, so);
      });
      asm volatile("s_waitcnt vmcnt(0)" : "+v"(in[0][0]), "+v"(in[0][1]), "+v"(in[0][2]), "+v"(in[1][0]), "+v"(in[1][1]), "+v"(in[1][2]), "+v"(in[2][0]), "+v"(in[2][1]), "+v"(in[2][2])::"memory");
#define OUT_AUX 18
      const srd_t sout = mksrd(p.out, NB * SQ * 2 * DM * 2);
      const unsigned ob = ((b * SQ + 32 * w + 8 * hq + (r & 7)) * (2 * DM) + br * DM + 160 * gi + 16 * grp + 8 * hh) * 2;
#pragma unroll
      for (int i = 0; i < 3; ++i) {
        u32x4 res;
#pragma unroll
        for (int e = 0; e < 4; ++e) {
          float lo = mine[i][e >> 1][2 * (e & 1)], hi = mine[i][e >> 1][2 * (e & 1) + 1];
#pragma unroll
          for (int sr = 0; sr < 3; ++sr) {
            const unsigned v = __builtin_bit_cast(u32x4, in[sr][i])[e];
            lo += __uint_as_float(v << 16); hi += __uint_as_float(v & 0xffff0000u);
          }
          res[e] = cvtpk(lo, hi);
        }
        __builtin_amdgcn_raw_buffer_store_b128(res, sout, (i < 2 || grp < 2) ? ob + i * 128 : OOB, 0, OUT_AUX);
      }
    }
  }
}

extern "C" void kernel_launch(void* const* d_in, const int* in_sizes, int n_in, void* d_out, int out_size, void* d_ws, size_t ws_size, hipStream_t stream) {
  Params p;
  for (int i = 0; i < 2; ++i) { p.x[i] = d_in[5 * i]; p.k[i] = d_in[5 * i + 1]; p.v[i] = d_in[5 * i + 2]; p.wq[i] = d_in[5 * i + 3]; p.wo[i] = d_in[5 * i + 4]; }
  p.ows = d_ws;
  p.part = (float*)((char*)d_ws + 25165824);
  p.out = d_out;
  p.sync = (unsigned*)((char*)d_ws + 25165824 + 10485760);
  (void)hipFuncSetAttribute((const void*)k1_attn, hipFuncAttributeMaxDynamicSharedMemorySize, 163840);
  k1_attn<<<256, 512, 163840, stream>>>(p);
}
```
